```python
import torch

def _softmax(x, axis=-1):
    return torch.softmax(x, dim=axis)
def _gelu(x):
    return torch.nn.functional.gelu(x, approximate="tanh")
def _silu(x):
    return torch.nn.functional.silu(x)
def _sigmoid(x):
    return torch.sigmoid(x)
def _layernorm(x, axis=-1, eps=1e-5):
    m = x.mean(dim=axis, keepdim=True)
    v = x.var(dim=axis, keepdim=True, unbiased=False)
    return (x - m) / torch.sqrt(v + eps)
def _scatter_add(tgt, idx, src, axis=0):
    return tgt.clone().index_add_(axis, idx, src)
def _segment_sum(src, seg, n):
    out = torch.zeros((n,) + src.shape[1:], dtype=src.dtype)
    return out.index_add_(0, seg, src)
def _pad(x, pw):
    flat = tuple(v for pair in reversed(pw) for v in pair)
    return torch.nn.functional.pad(x, flat)

kernel_name = 'standalone__molecular_angular_gemnet__graph'

def setup_inputs():

    _w = (lambda a: (lambda t: t.to(torch.bfloat16) if t.is_floating_point() else t)(torch.as_tensor(a)))
    N, deg, De, L = 10000, 32, 4, 3
    E = N * deg
    T = E * deg
    NR = 8
    dist = torch.rand(E).to(float)
    angle = (torch.rand(T) * torch.pi).to(float)
    centers = torch.linspace(0.0, 1.0, NR).to(float)
    ang_freq = torch.arange(1, L + 1).to(float)
    return {
        "m": _w((lambda *s: torch.randn(*s, dtype=torch.bfloat16) * 0.05)(E, De)),
        "idx_in": _w((lambda lo, hi, *s: torch.randint(lo, hi, s))(0, E, T).to(torch.int64)),
        "idx_out": _w((lambda lo, hi, *s: torch.randint(lo, hi, s))(0, E, T).to(torch.int64)),
        "dist": _w(dist),
        "angle": _w(angle),
        "centers": _w(centers),
        "ang_freq": _w(ang_freq),
        "w_rbf": _w((lambda *s: torch.randn(*s, dtype=torch.bfloat16) * 0.2)(NR, De)),
        "w_sbf": _w((lambda *s: torch.randn(*s, dtype=torch.bfloat16) * 0.2)(NR * L, De)),
        "w_down": _w((lambda *s: torch.randn(*s, dtype=torch.bfloat16) * 0.2)(De, De)),
        "w_up": _w((lambda *s: torch.randn(*s, dtype=torch.bfloat16) * 0.2)(De, De)),
    }

def reference(m, idx_in, idx_out, dist, angle, centers, ang_freq, w_rbf, w_sbf, w_down, w_up):
    E, De = m.shape[0], m.shape[1]
    NR = centers.shape[0]
    L = ang_freq.shape[0]
    rbf = torch.exp(-10.0 * (dist[:, None] - centers[None, :]) ** 2)
    cbf = torch.cos(ang_freq[None, :] * angle[:, None])
    rbf_t = rbf[idx_out]
    sbf = torch.reshape(rbf_t[:, :, None] * cbf[:, None, :], (-1, NR * L))
    x = (m[idx_in] @ w_down) * (sbf @ w_sbf)
    agg = _segment_sum(x, idx_out, E)
    return m + (agg @ w_up) * (rbf @ w_rbf)
```

```cpp
#include <hip/hip_runtime.h>
#include <hip/hip_bf16.h>

typedef unsigned int u32;
typedef unsigned long long u64;
typedef unsigned short u16;
typedef u32 u32x2 __attribute__((ext_vector_type(2)));
typedef u32 u32x3 __attribute__((ext_vector_type(3)));
typedef u32 u32x4 __attribute__((ext_vector_type(4)));
typedef u16 u16x2 __attribute__((ext_vector_type(2)));
typedef float f32x2 __attribute__((ext_vector_type(2)));
typedef __bf16 bf16x2 __attribute__((ext_vector_type(2)));
typedef float f32x16 __attribute__((ext_vector_type(16)));
typedef __bf16 bf16x8 __attribute__((ext_vector_type(8)));
#define LDS __attribute__((address_space(3)))

#define LD_AUX 2
#define ST_AUX 2
#define RD_AUX 2

namespace {

constexpr u32 kE = 320000, kT = 10240000;
constexpr u32 kNB = 256, kBE = kE / kNB;
constexpr u32 kTile = 20480, kNT = kT / kTile;
constexpr u32 kTileBytes = (kTile + kNB) * 6;
constexpr u32 kNG = 157;
constexpr u32 kPB = kNT / 2, kPE = kE / kPB;
constexpr u32 kRows = 1320;

constexpr size_t kOffRec = 0;
constexpr size_t kOffDesc = (size_t)kNT * kTileBytes;
constexpr size_t kOffMd = kOffDesc + (size_t)kNT * kNB * 4;
constexpr size_t kOffMax = kOffMd + (size_t)kE * 8;
constexpr size_t kOffOvf = kOffMax + 65536;
constexpr u32 kOvfCap = kNT * (kTile + kNB);
constexpr size_t kOffEnd = kOffOvf + (size_t)kOvfCap * 8;

#define STAMP(k) do {} while (0)

__device__ __forceinline__ float bf_lo(u32 v) { return __uint_as_float(v << 16); }
__device__ __forceinline__ float bf_hi(u32 v) { return __uint_as_float(v & 0xffff0000u); }
__device__ __forceinline__ float bf1(u16 v) { return __uint_as_float((u32)v << 16); }
__device__ __forceinline__ u32 pk_bf16(float lo, float hi) {
    f32x2 v = {lo, hi};
    return __builtin_bit_cast(u32, __builtin_convertvector(v, bf16x2));
}
__device__ __forceinline__ float rnd_bf16(float x) { return __uint_as_float(pk_bf16(0.f, x)); }
__device__ __forceinline__ auto mk_rsrc(const void* p, u32 bytes) {
    return __builtin_amdgcn_make_buffer_rsrc((void*)p, 0, bytes, 0x00020000);
}
__device__ __forceinline__ u32 pk_max16(u32 a, u32 b) {
    return __builtin_bit_cast(u32, __builtin_elementwise_max(__builtin_bit_cast(u16x2, a), __builtin_bit_cast(u16x2, b)));
}
__device__ __forceinline__ u32 pk_subsat16(u32 a, u32 b) {
    return __builtin_bit_cast(u32, __builtin_elementwise_sub_sat(__builtin_bit_cast(u16x2, a), __builtin_bit_cast(u16x2, b)));
}

struct AngWin { u32 base; bool ok; };
constexpr u32 kAngSpan = 23u << 7;
__device__ __forceinline__ AngWin angle_window(u32 f0, u32 f1, u32 f2) {
    u32 fm = max(f0 & 0x7fffu, max(f1 & 0x7fffu, f2 & 0x7fffu));
    int ef = (int)((fm + 127u) >> 7) - 127;
    AngWin w;
    w.ok = ef <= 100;
    ef = min(max(ef, -100), 100);
    w.base = (u32)(114 - ef) << 7;
    return w;
}

#define FENCE() __builtin_amdgcn_sched_barrier(0)

struct Raw { u32x4 in, out; u32x2 an; };

__device__ __forceinline__ Raw load_chunk(const int* idx_in, const int* idx_out, const u16* angle, u32 first, u32 tid) {
    Raw r;
    r.out = __builtin_amdgcn_raw_buffer_load_b128(mk_rsrc(idx_out + first, 16384), tid * 16, 0, LD_AUX);
    r.in = __builtin_amdgcn_raw_buffer_load_b128(mk_rsrc(idx_in + first, 16384), tid * 16, 0, LD_AUX);
    r.an = __builtin_amdgcn_raw_buffer_load_b64(mk_rsrc(angle + first, 8192), tid * 8, 0, LD_AUX);
    FENCE();
    return r;
}

__device__ __forceinline__ void pack_hist(const Raw& r, u32* pay, u32* meta, u32 base2, u32& amax, LDS char* cnt) {
    u32 c01 = pk_subsat16(r.an.x & 0x7fff7fffu, base2);
    u32 c23 = pk_subsat16(r.an.y & 0x7fff7fffu, base2);
    amax = pk_max16(amax, pk_max16(c01, c23));
#pragma unroll
    for (int q = 0; q < 4; q++) {
        u32 o = r.out[q], i = r.in[q];
        u32 j = __umulhi(o, 3435974u);
        u32 ol = o - j * kBE;
        u32 c = q < 2 ? c01 : c23;
        u32 ac = (q & 1) ? (c << 4) & 0xfff00000u : c << 20;
        pay[q] = ac | (ol << 9) | (i & 511u);
        meta[q] = (((i << 9) + (16u << 16)) & 0xfffc0000u) | (j << 2);
        asm volatile("" : "+v"(pay[q]), "+v"(meta[q]));
        __hip_atomic_fetch_add((LDS u32*)(cnt + (j << 2)), 1u, __ATOMIC_RELAXED, __HIP_MEMORY_SCOPE_WORKGROUP);
    }
    FENCE();
}

__device__ __forceinline__ void scan_tile(LDS char* cnt, LDS char* stage, LDS u32* total, u32 flag, u32* desc, u32 lane) {
    const u32x4 c = *(LDS u32x4*)(cnt + lane * 16);
    const u32x4 np = (c + 1u) >> 1;
    const u32 s1 = np.x, s2 = s1 + np.y, s3 = s2 + np.z, tot = s3 + np.w;
    u32 inc = tot;
#pragma unroll
    for (int d = 1; d < 64; d <<= 1) {
        u32 v = __shfl_up(inc, d);
        if (lane >= (u32)d) inc += v;
    }
    const u32 ex = inc - tot;
    const u32x4 st = {ex, ex + s1, ex + s2, ex + s3};
    *(LDS u32x4*)(cnt + lane * 16) = st * 12u;
#pragma unroll
    for (int q = 0; q < 4; q++)
        if (c[q] & 1u) *(LDS u16*)(stage + (st[q] + np[q]) * 12u - 6u) = 0;
    if (lane == 63) *total = inc * 12u;
    *(u32x4*)(desc + lane * 4) = st | (np << 16) | flag;
}

template <int N>
__device__ __forceinline__ void scatter(const u32* pay, const u32* meta, LDS char* cnt, LDS char* stage) {
    u32 x[N];
#pragma unroll
    for (int k = 0; k < N; k++)
        x[k] = __hip_atomic_fetch_add((LDS u32*)(cnt + (meta[k] & 0xffffu)), 6u, __ATOMIC_RELAXED, __HIP_MEMORY_SCOPE_WORKGROUP);
#pragma unroll
    for (int k = 0; k < N; k++) {
        u32 t = x[k] & 2u;
        *(LDS u32*)(stage + (x[k] + t)) = pay[k];
        *(LDS u16*)(stage + (x[k] - t - t) + 4) = (u16)(meta[k] >> 16);
    }
    FENCE();
}

__device__ __forceinline__ void write_tile(LDS char* stage, LDS u32* total, char* dst, u32 tid) {
    u32x4 v[8];
    const u32 tail = (__builtin_amdgcn_readfirstlane(*total) - 7 * 16384 + 127u) & ~127u;
#pragma unroll
    for (int k = 0; k < 8; k++) v[k] = *(LDS u32x4*)(stage + (k < 7 ? tid : min(tid, (kTileBytes - 7 * 16384) / 16 - 1)) * 16 + k * 16384);
#pragma unroll
    for (int k = 0; k < 8; k++)
        __builtin_amdgcn_raw_buffer_store_b128(v[k], mk_rsrc(dst + k * 16384, k < 7 ? 16384 : tail), tid * 16, 0, ST_AUX);
    FENCE();
}

__global__ void __launch_bounds__(1024) k_partition(const u16* __restrict__ m, const int* __restrict__ idx_in,
                                                    const int* __restrict__ idx_out, const u16* __restrict__ angle,
                                                    const u16* __restrict__ freq, const u16* __restrict__ w_down, char* ws) {
    __shared__ __attribute__((aligned(16))) char smem_[2560 + kTileBytes];
    LDS char* cnt0 = (LDS char*)smem_;
    LDS char* cnt1 = cnt0 + 1024;
    LDS u32* misc = (LDS u32*)(cnt1 + 1024);
    LDS char* stage = cnt0 + 2560;
    const u32 tid = threadIdx.x, lane = tid & 63, b = blockIdx.x;
    const u32 t0 = 2 * b * kTile;
    STAMP(0);
    const u32 f0 = freq[0], f1 = freq[1], f2 = freq[2];
    const u32x4 wda = *(const u32x4*)w_down, wdb = *(const u32x4*)(w_down + 8);
    const u32x4 mv = __builtin_amdgcn_raw_buffer_load_b128(mk_rsrc(m + (size_t)b * kPE * 4, kPE * 8), tid * 16, 0, 0);
    FENCE();
    Raw ra = load_chunk(idx_in, idx_out, angle, t0, tid), rb;
    if (tid < 640) *(LDS u32*)(cnt0 + tid * 4) = 0;
    const u32 base2 = angle_window(f0, f1, f2).base * 0x10001u;
    __syncthreads();
    if (tid < kPE / 2) {
        float wd[16];
#pragma unroll
        for (int k = 0; k < 4; k++) {
            wd[2 * k] = bf_lo(wda[k]); wd[2 * k + 1] = bf_hi(wda[k]);
            wd[8 + 2 * k] = bf_lo(wdb[k]); wd[9 + 2 * k] = bf_hi(wdb[k]);
        }
        u32x4 o;
#pragma unroll
        for (int h = 0; h < 2; h++) {
            float a0 = bf_lo(mv[2 * h]), a1 = bf_hi(mv[2 * h]), a2 = bf_lo(mv[2 * h + 1]), a3 = bf_hi(mv[2 * h + 1]);
            float r[4];
#pragma unroll
            for (int d = 0; d < 4; d++) r[d] = fmaf(a3, wd[12 + d], fmaf(a2, wd[8 + d], fmaf(a1, wd[4 + d], a0 * wd[d])));
            o[2 * h] = pk_bf16(r[0], r[1]);
            o[2 * h + 1] = pk_bf16(r[2], r[3]);
        }
        *(u32x4*)(ws + kOffMd + ((size_t)b * kPE + tid * 2) * 8) = o;
        u32 mx0 = pk_max16(o.x & 0x7fff7fffu, o.z & 0x7fff7fffu);
        u32 mx1 = pk_max16(o.y & 0x7fff7fffu, o.w & 0x7fff7fffu);
#pragma unroll
        for (int d = 1; d < 64; d <<= 1) {
            mx0 = pk_max16(mx0, __shfl_xor(mx0, d));
            mx1 = pk_max16(mx1, __shfl_xor(mx1, d));
        }
        if (lane == 0) { misc[2 + (tid >> 6) * 2] = mx0; misc[3 + (tid >> 6) * 2] = mx1; }
    }
    FENCE();

    u32 p0[20], m0[20], p1[20], m1[20], amax = 0;
    rb = load_chunk(idx_in, idx_out, angle, t0 + 4096, tid);
    pack_hist(ra, p0, m0, base2, amax, cnt0);
    ra = load_chunk(idx_in, idx_out, angle, t0 + 2 * 4096, tid);
    pack_hist(rb, p0 + 4, m0 + 4, base2, amax, cnt0);
    rb = load_chunk(idx_in, idx_out, angle, t0 + 3 * 4096, tid);
    pack_hist(ra, p0 + 8, m0 + 8, base2, amax, cnt0);
    ra = load_chunk(idx_in, idx_out, angle, t0 + 4 * 4096, tid);
    pack_hist(rb, p0 + 12, m0 + 12, base2, amax, cnt0);
    rb = load_chunk(idx_in, idx_out, angle, t0 + 5 * 4096, tid);
    pack_hist(ra, p0 + 16, m0 + 16, base2, amax, cnt0);
    if (max(amax & 0xffffu, amax >> 16) >= kAngSpan) misc[0] = 1;
    __syncthreads();
    STAMP(1);
    u32* desc = (u32*)(ws + kOffDesc);
    if (tid < 64) scan_tile(cnt0, stage, misc + 1, misc[0] << 15, desc + (2 * b) * kNB, lane);
    if (tid == 64) {
        u32 mx0 = 0, mx1 = 0;
#pragma unroll
        for (int w = 0; w < 10; w++) { mx0 = pk_max16(mx0, misc[2 + w * 2]); mx1 = pk_max16(mx1, misc[3 + w * 2]); }
        u32x2 o = {mx0, mx1};
        *(u32x2*)(ws + kOffMax + b * 8) = o;
    }
    __syncthreads();
    FENCE();
    ra = load_chunk(idx_in, idx_out, angle, t0 + 6 * 4096, tid);
    scatter<7>(p0, m0, cnt0, stage);
    pack_hist(rb, p1, m1, base2, amax, cnt1);
    rb = load_chunk(idx_in, idx_out, angle, t0 + 7 * 4096, tid);
    scatter<7>(p0 + 7, m0 + 7, cnt0, stage);
    pack_hist(ra, p1 + 4, m1 + 4, base2, amax, cnt1);
    ra = load_chunk(idx_in, idx_out, angle, t0 + 8 * 4096, tid);
    scatter<6>(p0 + 14, m0 + 14, cnt0, stage);
    pack_hist(rb, p1 + 8, m1 + 8, base2, amax, cnt1);
    rb = load_chunk(idx_in, idx_out, angle, t0 + 9 * 4096, tid);
    pack_hist(ra, p1 + 12, m1 + 12, base2, amax, cnt1);
    __syncthreads();
    STAMP(2);
    write_tile(stage, misc + 1, ws + kOffRec + (size_t)(2 * b) * kTileBytes, tid);
    STAMP(3);
    pack_hist(rb, p1 + 16, m1 + 16, base2, amax, cnt1);
    if (max(amax & 0xffffu, amax >> 16) >= kAngSpan) misc[0] = 1;
    __syncthreads();
    STAMP(4);
    if (tid < 64) scan_tile(cnt1, stage, misc + 1, misc[0] << 15, desc + (2 * b + 1) * kNB, lane);
    __syncthreads();
    scatter<10>(p1, m1, cnt1, stage);
    scatter<10>(p1 + 10, m1 + 10, cnt1, stage);
    __syncthreads();
    STAMP(5);
    write_tile(stage, misc + 1, ws + kOffRec + (size_t)(2 * b + 1) * kTileBytes, tid);
    STAMP(6);
}

__device__ __forceinline__ u32x4 rbf_row(float dv, u32x4 cen) {
    float r[8];
#pragma unroll
    for (int k = 0; k < 8; k++) {
        float d = rnd_bf16(dv - ((k & 1) ? bf_hi(cen[k >> 1]) : bf_lo(cen[k >> 1])));
        float t = rnd_bf16(-10.0f * rnd_bf16(d * d));
        r[k] = __builtin_amdgcn_exp2f(t * 1.44269504088896340736f);
    }
    u32x4 o = {pk_bf16(r[0], r[1]), pk_bf16(r[2], r[3]), pk_bf16(r[4], r[5]), pk_bf16(r[6], r[7])};
    return o;
}

__device__ __forceinline__ u32x2 finish_edge(const float (&agg)[4], u32x2 mv, u32x2 hv, const u16* w_up) {
    float a[4], o[4];
#pragma unroll
    for (int k = 0; k < 4; k++) a[k] = rnd_bf16(agg[k]);
    const float mm[4] = {bf_lo(mv.x), bf_hi(mv.x), bf_lo(mv.y), bf_hi(mv.y)};
    const float hh[4] = {bf_lo(hv.x), bf_hi(hv.x), bf_lo(hv.y), bf_hi(hv.y)};
#pragma unroll
    for (int d = 0; d < 4; d++) {
        float u = fmaf(a[3], bf1(w_up[12 + d]), fmaf(a[2], bf1(w_up[8 + d]), fmaf(a[1], bf1(w_up[4 + d]), a[0] * bf1(w_up[d]))));
        o[d] = mm[d] + rnd_bf16(rnd_bf16(u) * hh[d]);
    }
    u32x2 r = {pk_bf16(o[0], o[1]), pk_bf16(o[2], o[3])};
    return r;
}

__device__ __forceinline__ void slow_bucket(LDS char* sm, u32 e0, u32 ne, u32 T, const u16* m, const int* idx_in, const int* idx_out,
                                            const u16* dist, const u16* angle, const u16* centers, const u16* freq,
                                            const u16* w_rbf, const u16* w_sbf, const u16* w_down, const u16* w_up, u16* out) {
    LDS float* agg = (LDS float*)sm;
    LDS float* A = agg + kBE * 4;
    const u32 tid = threadIdx.x;
    for (u32 e = tid; e < ne; e += 1024) {
        u32x4 rv = rbf_row(bf1(dist[e0 + e]), *(const u32x4*)centers);
#pragma unroll 1
        for (int v = 0; v < 12; v++) {
            float a = 0.f;
#pragma unroll
            for (int r = 0; r < 8; r++) a = fmaf((r & 1) ? bf_hi(rv[r >> 1]) : bf_lo(rv[r >> 1]), bf1(w_sbf[r * 12 + v]), a);
            A[e * 12 + v] = a;
        }
#pragma unroll
        for (int d = 0; d < 4; d++) agg[e * 4 + d] = 0.f;
    }
    __syncthreads();
    for (u32 t = tid; t < T; t += 1024) {
        u32 e = (u32)idx_out[t] - e0;
        if (e < ne) {
            u32x2 mv = *(const u32x2*)(m + (size_t)idx_in[t] * 4);
            float av = bf1(angle[t]);
            float g[4] = {0.f, 0.f, 0.f, 0.f};
#pragma unroll 1
            for (int l = 0; l < 3; l++) {
                float c = cosf(rnd_bf16(bf1(freq[l]) * av));
#pragma unroll
                for (int d = 0; d < 4; d++) g[d] = fmaf(c, A[e * 12 + l * 4 + d], g[d]);
            }
#pragma unroll
            for (int d = 0; d < 4; d++) {
                float md = fmaf(bf_hi(mv.y), bf1(w_down[12 + d]), fmaf(bf_lo(mv.y), bf1(w_down[8 + d]),
                           fmaf(bf_hi(mv.x), bf1(w_down[4 + d]), bf_lo(mv.x) * bf1(w_down[d]))));
                __hip_atomic_fetch_add(agg + e * 4 + d, rnd_bf16(md) * g[d], __ATOMIC_RELAXED, __HIP_MEMORY_SCOPE_WORKGROUP);
            }
        }
    }
    __syncthreads();
    for (u32 e = tid; e < ne; e += 1024) {
        u32x4 rv = rbf_row(bf1(dist[e0 + e]), *(const u32x4*)centers);
        float h[4], a[4];
#pragma unroll
        for (int d = 0; d < 4; d++) {
            float s = 0.f;
#pragma unroll
            for (int r = 0; r < 8; r++) s = fmaf((r & 1) ? bf_hi(rv[r >> 1]) : bf_lo(rv[r >> 1]), bf1(w_rbf[r * 4 + d]), s);
            h[d] = s;
            a[d] = agg[e * 4 + d];
        }
        u32x2 hv = {pk_bf16(h[0], h[1]), pk_bf16(h[2], h[3])};
        *(u32x2*)(out + (size_t)(e0 + e) * 4) = finish_edge(a, *(const u32x2*)(m + (size_t)(e0 + e) * 4), hv, w_up);
    }
}

constexpr u32 kBinOff = 2560;
constexpr u32 kMiscOff = kBinOff + kNG * 1024;
constexpr u32 kAggOff = kRows * 16;
constexpr u32 kAggHalf = kRows * 8;
constexpr u32 kParkOff = kAggOff + 8 * kAggHalf;
constexpr float kMagic = 12582912.0f;
constexpr u64 kUnbias = 0ull - 0x4B4000004B400000ull;

struct Consts { float f0, f1, f2; u32 abase; u32 aggb; bf16x8 w; };

__device__ __forceinline__ void accumulate(u32 p, u32x2 md, const Consts& c, LDS char* sm) {
    const u32 o8 = (p >> 6) & 0x3ff8u;
    const u32x4 rb = *(LDS u32x4*)(sm + o8 + o8);
    const float av = __uint_as_float(((p >> 4) & 0x0fff0000u) + c.abase);
    const float c0 = __builtin_amdgcn_cosf(rnd_bf16(c.f0 * av) * 0.15915494309189535f);
    const float c1 = __builtin_amdgcn_cosf(rnd_bf16(c.f1 * av) * 0.15915494309189535f);
    const float c2 = __builtin_amdgcn_cosf(rnd_bf16(c.f2 * av) * 0.15915494309189535f);
    const f32x16 z = {0, 0, 0, 0, 0, 0, 0, 0, 0, 0, 0, 0, 0, 0, 0, 0};
    const f32x16 A = __builtin_amdgcn_mfma_f32_32x32x16_bf16(c.w, __builtin_bit_cast(bf16x8, rb), z, 0, 0, 0);
    float g[4];
#pragma unroll
    for (int d = 0; d < 4; d++) g[d] = fmaf(c2, A[8 + d], fmaf(c1, A[4 + d], c0 * A[d]));
    u32x2 xa = {__float_as_uint(fmaf(bf_lo(md.x), g[0], kMagic)), __float_as_uint(fmaf(bf_hi(md.x), g[1], kMagic))};
    u32x2 xb = {__float_as_uint(fmaf(bf_lo(md.y), g[2], kMagic)), __float_as_uint(fmaf(bf_hi(md.y), g[3], kMagic))};
    LDS char* ap = sm + (o8 + c.aggb);
    __hip_atomic_fetch_add((LDS u64*)ap, __builtin_bit_cast(u64, xa) + kUnbias, __ATOMIC_RELAXED, __HIP_MEMORY_SCOPE_WORKGROUP);
    __hip_atomic_fetch_add((LDS u64*)(ap + kAggHalf), __builtin_bit_cast(u64, xb) + kUnbias, __ATOMIC_RELAXED, __HIP_MEMORY_SCOPE_WORKGROUP);
}

template <typename R>
__device__ __forceinline__ void spill(u64 ov, u32 pay, u32 w4, R rov, u32& nov) {
    const u32 pos = __builtin_amdgcn_mbcnt_hi((u32)(ov >> 32), __builtin_amdgcn_mbcnt_lo((u32)ov, nov));
    const u32x2 r = {pay, w4};
    if (__builtin_amdgcn_inverse_ballot_w64(ov)) __builtin_amdgcn_raw_buffer_store_b64(r, rov, pos * 8, 0, 0);
    nov += __builtin_popcountll(ov);
}

template <typename R>
__device__ __forceinline__ void bin_pair(u32x3 v, u32 c16, u32 c1023, R rov, u32& nov) {
    u32 wa, wb, ra, rb, ta, tb;
    u64 ma, mb, mt;
    asm volatile(
        "v_and_b32 %[wa], 0xffc, %[y]\n\t"
        "v_lshrrev_b32 %[wb], 16, %[y]\n\t"
        "v_cmp_ne_u32 %[ma], 0, %[wa]\n\t"
        "v_and_b32 %[wb], 0xffc, %[wb]\n\t"
        "v_cmp_ne_u32 %[mb], 0, %[wb]\n\t"
        "s_mov_b64 exec, %[ma]\n\t"
        "ds_add_rtn_u32 %[ra], %[wa], %[c16]\n\t"
        "s_mov_b64 exec, %[mb]\n\t"
        "ds_add_rtn_u32 %[rb], %[wb], %[c16]\n\t"
        "s_mov_b64 exec, -1\n\t"
        "v_lshl_or_b32 %[ta], %[wa], 6, %[c1023]\n\t"
        "v_lshl_or_b32 %[tb], %[wb], 6, %[c1023]\n\t"
        "s_waitcnt lgkmcnt(1)\n\t"
        "v_cmp_le_u32 vcc, %[ra], %[ta]\n\t"
        "s_waitcnt lgkmcnt(0)\n\t"
        "v_cmp_le_u32 %[mt], %[rb], %[tb]\n\t"
        "s_and_b64 exec, vcc, %[ma]\n\t"
        "ds_write_b32 %[ra], %[x] offset:%[off]\n\t"
        "s_andn2_b64 %[ma], %[ma], vcc\n\t"
        "s_and_b64 exec, %[mt], %[mb]\n\t"
        "ds_write_b32 %[rb], %[z] offset:%[off]\n\t"
        "s_andn2_b64 %[mb], %[mb], %[mt]\n\t"
        "s_mov_b64 exec, -1"
        : [wa] "=&v"(wa), [wb] "=&v"(wb), [ra] "=&v"(ra), [rb] "=&v"(rb), [ta] "=&v"(ta), [tb] "=&v"(tb),
          [ma] "=&s"(ma), [mb] "=&s"(mb), [mt] "=&s"(mt)
        : [x] "v"(v.x), [y] "v"(v.y), [z] "v"(v.z), [c16] "v"(c16), [c1023] "v"(c1023), [off] "i"(kBinOff - 1024)
        : "vcc", "scc", "memory");
    if (ma | mb) {
        spill(ma, v.x, wa, rov, nov);
        spill(mb, v.z, wb, rov, nov);
    }
}

__device__ __forceinline__ void drain_rows(LDS char* sm, u32 e, float (&acc)[4], bool clear) {
#pragma unroll
    for (int k = 0; k < 4; k++) {
#pragma unroll
        for (int h = 0; h < 2; h++) {
            LDS u32x2* p = (LDS u32x2*)(sm + kAggOff + (2 * k + h) * kAggHalf + e * 8);
            u32x2 v = *p;
            acc[2 * h] += (float)(int)v.x;
            acc[2 * h + 1] += (float)(int)(v.y + (v.x >> 31));
            if (clear) { u32x2 z = {0, 0}; *p = z; }
        }
    }
}

__global__ void __launch_bounds__(1024) k_accumulate(const u16* __restrict__ m, const int* __restrict__ idx_in,
                                                     const int* __restrict__ idx_out, const u16* __restrict__ dist,
                                                     const u16* __restrict__ angle, const u16* __restrict__ centers,
                                                     const u16* __restrict__ freq, const u16* __restrict__ w_rbf,
                                                     const u16* __restrict__ w_sbf, const u16* __restrict__ w_down,
                                                     const u16* __restrict__ w_up, u16* __restrict__ out, char* ws) {
    __shared__ __attribute__((aligned(16))) char smem_[163840];
    LDS char* sm = (LDS char*)smem_;
    LDS u32* misc = (LDS u32*)(sm + kMiscOff);
    const u32 tid = threadIdx.x, lane = tid & 63;
    const u32 wave = __builtin_amdgcn_readfirstlane(tid >> 6);
    const u32 j = (blockIdx.x & 7) * 32 + (blockIdx.x >> 3);
    const u32 e0 = j * kBE;
    STAMP(7);

    const u32 dsc = __builtin_amdgcn_raw_buffer_load_b32(mk_rsrc(ws + kOffDesc, kNT * kNB * 4), ((lane * 16 + wave) * kNB + j) * 4, 0, 0);
    auto rmx = mk_rsrc(ws + kOffMax, kPB * 8);
    const u32x4 mxa = __builtin_amdgcn_raw_buffer_load_b128(rmx, lane * 32, 0, 0);
    const u32x4 mxb = __builtin_amdgcn_raw_buffer_load_b128(rmx, lane * 32 + 16, 0, 0);
    const bool own = tid < kBE / 2;
    const u32 dd = __builtin_amdgcn_raw_buffer_load_b32(mk_rsrc(dist + e0, kBE * 2), tid * 4, 0, 0);
    const u32x4 mv = __builtin_amdgcn_raw_buffer_load_b128(mk_rsrc(m + (size_t)e0 * 4, kBE * 8), tid * 16, 0, 0);
    const u32 fq0 = freq[0], fq1 = freq[1], fq2 = freq[2];
    const u32x4 cen = *(const u32x4*)centers;
    const u32 row = lane & 31, vv = (row & 3) + 4 * (row >> 3);
    const bool wlive = ((row >> 2) & 1) == (lane >> 5);
    u16 wraw[8];
#pragma unroll
    for (int r = 0; r < 8; r++) wraw[r] = vv < 12 ? w_sbf[r * 12 + vv] : w_rbf[r * 4 + (vv & 3)];
    FENCE();

    {
        const u32 s = (kBE + lane) << 9;
        const u32x4 sv = {s, s, s, s};
#pragma unroll
        for (int k = 0; k < 10; k++)
            *(LDS u32x4*)(sm + kBinOff + min(wave + k * 16, kNG - 1) * 1024 + lane * 16) = sv;
        if (tid < kNG * 4 + 4) *(LDS u32*)(sm + tid * 4) = (tid >> 2) * 1024 + (tid & 3) * 4;
    }
    FENCE();

    const char* recs = ws + kOffRec;
    constexpr int D = 6;
    u32x3 buf[D];
    const u32 sbeg = min(lane * 16 + wave, kNT - 1) * kTileBytes + (dsc & 0x7fffu) * 12;
    const u32 send = sbeg + (dsc >> 16) * 12;
    auto issue = [&](int k) {
        buf[k % D] = __builtin_amdgcn_raw_buffer_load_b96(mk_rsrc(recs, __builtin_amdgcn_readlane(send, k)), lane * 12,
                                                          __builtin_amdgcn_readlane(sbeg, k), RD_AUX);
    };
#pragma unroll
    for (int k = 0; k < 4; k++) issue(k);
    FENCE();

    {
        u32 s = (dsc & 0x7fffu) * 2, n = (dsc >> 16) * 2;
#pragma unroll
        for (int d = 1; d < 32; d <<= 1) { s += __shfl_xor(s, d); n += __shfl_xor(n, d); }
        const bool fl = __any(dsc & 0x8000u);
        if (lane == 0) { misc[wave] = s | (fl ? 0x80000000u : 0u); misc[16 + wave] = n; }
    }

    u32 mx0 = pk_max16(pk_max16(mxa.x, mxa.z), pk_max16(mxb.x, mxb.z));
    u32 mx1 = pk_max16(pk_max16(mxa.y, mxa.w), pk_max16(mxb.y, mxb.w));
#pragma unroll
    for (int d = 1; d < 64; d <<= 1) {
        mx0 = pk_max16(mx0, __shfl_xor(mx0, d));
        mx1 = pk_max16(mx1, __shfl_xor(mx1, d));
    }
    const AngWin aw = angle_window(fq0, fq1, fq2);
    bool fast = aw.ok;
    float mp[4];
    {
        const u32 mb[4] = {mx0 & 0xffffu, mx0 >> 16, mx1 & 0xffffu, mx1 >> 16};
#pragma unroll
        for (int d = 0; d < 4; d++) {
            u32 ex = mb[d] ? (mb[d] + 127u) >> 7 : 127u;
            fast = fast && ex >= 97u && ex <= 157u;
            mp[d] = __uint_as_float(ex << 23);
        }
    }
    Consts cs;
    {
        const float sd = (vv & 2) ? ((vv & 1) ? mp[3] : mp[2]) : ((vv & 1) ? mp[1] : mp[0]);
        const float sc = vv < 12 ? __uint_as_float((254u << 23) - __float_as_uint(sd)) : 1.0f;
        u32 wmax = 0, wmin = 0xffffu;
        u32x4 wq;
#pragma unroll
        for (int r = 0; r < 4; r++) {
            wq[r] = wlive ? pk_bf16(bf1(wraw[2 * r]) * sc, bf1(wraw[2 * r + 1]) * sc) : 0u;
            const u32 a = wraw[2 * r] & 0x7fffu, b = wraw[2 * r + 1] & 0x7fffu;
            wmax = max(wmax, max(a, b));
            wmin = min(wmin, min(a ? a : 0xffffu, b ? b : 0xffffu));
        }
        fast = fast && !__any(wmax >= (157u << 7) || wmin < (67u << 7));
        cs.w = __builtin_bit_cast(bf16x8, wq);
        cs.f0 = bf_lo(fq0); cs.f1 = bf_lo(fq1); cs.f2 = bf_lo(fq2);
        cs.abase = aw.base << 16;
        cs.aggb = kAggOff + (wave >> 2) * 2 * kAggHalf;
        asm volatile("" : "+v"(cs.f0), "+v"(cs.f1), "+v"(cs.f2), "+v"(cs.abase), "+v"(cs.aggb));
    }
    u32x4 rbs[2];
    u32x2 hv[2];
    float inv[2];
    if (wave < 10) {
#pragma unroll
        for (int q = 0; q < 2; q++) {
            const float dv = q ? bf_hi(dd) : bf_lo(dd);
            u32x4 rv = rbf_row(dv, cen);
            const bool bad = ((rv.x | rv.y | rv.z | rv.w) & 0x40004000u) != 0;
            if (bad) rv = u32x4{0, 0, 0, 0};
            const f32x16 z = {0, 0, 0, 0, 0, 0, 0, 0, 0, 0, 0, 0, 0, 0, 0, 0};
            const f32x16 A = __builtin_amdgcn_mfma_f32_32x32x16_bf16(cs.w, __builtin_bit_cast(bf16x8, rv), z, 0, 0, 0);
            float G = 0.f;
#pragma unroll
            for (int d = 0; d < 4; d++) G = fmaxf(G, (fabsf(A[d]) + fabsf(A[4 + d]) + fabsf(A[8 + d])) * mp[d]);
            const u32 eg = max((__float_as_uint(G) >> 23) + 1u, 84u);
            const float sc = __uint_as_float((271u - eg) << 23);
            inv[q] = __uint_as_float((eg - 17u) << 23);
#pragma unroll
            for (int r = 0; r < 4; r++) rbs[q][r] = pk_bf16(bf_lo(rv[r]) * sc, bf_hi(rv[r]) * sc);
            hv[q].x = bad ? 0x7fc07fc0u : pk_bf16(A[12], A[13]);
            hv[q].y = bad ? 0x7fc07fc0u : pk_bf16(A[14], A[15]);
        }
    }
    __syncthreads();
    STAMP(8);
    u32 obase, wpre;
    {
        u32 v = misc[lane & 15];
        const u32 n = misc[16 + (lane & 15)];
        fast = fast && !__any(v >> 31);
        v &= 0x7fffffffu;
        wpre = n;
#pragma unroll
        for (int d = 1; d < 16; d <<= 1) {
            v += __shfl_xor(v, d);
            const u32 u = __shfl_up(wpre, d, 16);
            if ((lane & 15) >= (u32)d) wpre += u;
        }
        wpre -= n;
        obase = __builtin_amdgcn_readfirstlane(v);
    }
    if (!__builtin_amdgcn_readfirstlane(fast)) {
        __syncthreads();
        slow_bucket(sm, e0, kBE, kT, m, idx_in, idx_out, dist, angle, centers, freq, w_rbf, w_sbf, w_down, w_up, out);
        return;
    }
    auto rov = mk_rsrc(ws + kOffOvf + (size_t)obase * 8, (kOvfCap - obase) * 8);
    const u32 nov0 = __builtin_amdgcn_readlane(wpre, wave);
    u32 nov = nov0;

    u32 c16 = 16, c1023 = 1023;
    asm volatile("" : "+v"(c16), "+v"(c1023));
#pragma unroll
    for (int k = 4; k < D; k++) issue(k);
#pragma unroll
    for (int k = 0; k < 32; k++) {
        bin_pair(buf[k % D], c16, c1023, rov, nov);
        if (k + D < 32) issue(k + D);
    }
    if (__any((dsc >> 16) > 64u)) {
#pragma unroll 1
        for (int k = 0; k < 32; k++) {
            const u32 e = __builtin_amdgcn_readlane(send, k);
#pragma unroll 1
            for (u32 c = __builtin_amdgcn_readlane(sbeg, k) + 768; c < e; c += 768)
                bin_pair(__builtin_amdgcn_raw_buffer_load_b96(mk_rsrc(recs, e), lane * 12, c, RD_AUX), c16, c1023, rov, nov);
        }
    }
    asm volatile("s_waitcnt lgkmcnt(0)" ::: "memory");
    if (lane == 0) misc[32 + wave] = nov - nov0;
    __syncthreads();
    STAMP(9);
    u32x4 P[10];
#pragma unroll
    for (int k = 0; k < 10; k++) P[k] = *(LDS u32x4*)(sm + kBinOff + min(wave + k * 16, kNG - 1) * 1024 + lane * 16);
    auto rmd = mk_rsrc(ws + kOffMd, kE * 8);
    auto gather = [&](int s) {
        return __builtin_amdgcn_raw_buffer_load_b64(rmd, (P[s >> 2][s & 3] & 511u) << 3, ((wave + (s >> 2) * 16) * 4 + (s & 3)) * 4096, 0);
    };
    constexpr int PF = 4;
    u32x2 md[PF];
#pragma unroll
    for (int s = 0; s < PF; s++) md[s] = gather(s);
    __syncthreads();
    {
        const u32x4 z = {0, 0, 0, 0};
#pragma unroll
        for (int k = 0; k < 6; k++)
            if (tid + k * 1024 < 8 * kAggHalf / 16) *(LDS u32x4*)(sm + kAggOff + (tid + k * 1024) * 16) = z;
        if (own) {
            *(LDS u32x4*)(sm + tid * 32) = rbs[0];
            *(LDS u32x4*)(sm + tid * 32 + 16) = rbs[1];
            u32x4 hh = {hv[0].x, hv[0].y, hv[1].x, hv[1].y};
            u32x2 iv = {__float_as_uint(inv[0]), __float_as_uint(inv[1])};
            *(LDS u32x4*)(sm + kParkOff + tid * 16) = mv;
            *(LDS u32x4*)(sm + kParkOff + 10240 + tid * 16) = hh;
            *(LDS u32x2*)(sm + kParkOff + 20480 + tid * 8) = iv;
        } else if (tid < kRows / 2) {
            *(LDS u32x4*)(sm + tid * 32) = z;
            *(LDS u32x4*)(sm + tid * 32 + 16) = z;
        }
    }
    __syncthreads();
    STAMP(10);
#pragma unroll
    for (int s = 0; s < 36; s++) {
        const u32x2 cur = md[s % PF];
        md[s % PF] = gather(s + PF);
        accumulate(P[s >> 2][s & 3], cur, cs, sm);
    }
    if (wave + 144 < kNG) {
#pragma unroll
        for (int s = 36; s < 40; s++) accumulate(P[s >> 2][s & 3], md[s % PF], cs, sm);
    }
    STAMP(11);
    float acc[2][4] = {{0.f, 0.f, 0.f, 0.f}, {0.f, 0.f, 0.f, 0.f}};
    {
        const u32 cn = misc[32 + (lane & 15)];
        u32 inc = cn;
#pragma unroll
        for (int d = 1; d < 16; d <<= 1) {
            const u32 u = __shfl_up(inc, d, 16);
            if ((lane & 15) >= (u32)d) inc += u;
        }
        const u32 novf = __builtin_amdgcn_readlane(inc, 15);
        const u32 delta = wpre - (inc - cn);
        u32 it = 0;
#pragma unroll 1
        for (u32 i0 = 0; i0 < novf; i0 += 1024) {
            if (++it == 20) {
                it = 0;
                __syncthreads();
                if (own) { drain_rows(sm, tid * 2, acc[0], true); drain_rows(sm, tid * 2 + 1, acc[1], true); }
                __syncthreads();
            }
            if (i0 + (15 - wave) * 64 < novf) {
                const u32 i = i0 + (15 - wave) * 64 + lane;
                u32 src = 0;
#pragma unroll
                for (int w = 0; w < 15; w++) src += i >= (u32)__builtin_amdgcn_readlane(inc, w) ? 1u : 0u;
                const u32 pos = i + __shfl(delta, src);
                u32x2 r = __builtin_amdgcn_raw_buffer_load_b64(rov, i < novf ? pos * 8 : 0xf0000000u, 0, 0);
                if (i >= novf) r.x = (kBE + lane) << 9;
                const u32x2 g = __builtin_amdgcn_raw_buffer_load_b64(rmd, ((r.x & 511u) << 3) + ((r.y - 16u) << 10), 0, 0);
                accumulate(r.x, g, cs, sm);
            }
        }
    }
    __syncthreads();
    STAMP(12);
    if (own) {
        drain_rows(sm, tid * 2, acc[0], false);
        drain_rows(sm, tid * 2 + 1, acc[1], false);
        const u32x4 m2 = *(LDS u32x4*)(sm + kParkOff + tid * 16);
        const u32x4 hh = *(LDS u32x4*)(sm + kParkOff + 10240 + tid * 16);
        const u32x2 iv = *(LDS u32x2*)(sm + kParkOff + 20480 + tid * 8);
        u32x4 o;
#pragma unroll
        for (int q = 0; q < 2; q++) {
            float a[4];
#pragma unroll
            for (int d = 0; d < 4; d++) a[d] = acc[q][d] * __uint_as_float(iv[q]) * mp[d];
            u32x2 mq = {m2[2 * q], m2[2 * q + 1]}, hq = {hh[2 * q], hh[2 * q + 1]};
            u32x2 r = finish_edge(a, mq, hq, w_up);
            o[2 * q] = r.x;
            o[2 * q + 1] = r.y;
        }
        *(u32x4*)(out + ((size_t)e0 + tid * 2) * 4) = o;
    }
    STAMP(13);
}

__global__ void __launch_bounds__(1024) k_generic(u32 E, u32 T, const u16* __restrict__ m, const int* __restrict__ idx_in,
                                                  const int* __restrict__ idx_out, const u16* __restrict__ dist,
                                                  const u16* __restrict__ angle, const u16* __restrict__ centers,
                                                  const u16* __restrict__ freq, const u16* __restrict__ w_rbf,
                                                  const u16* __restrict__ w_sbf, const u16* __restrict__ w_down,
                                                  const u16* __restrict__ w_up, u16* __restrict__ out) {
    __shared__ __attribute__((aligned(16))) char smem_[kBE * 64];
    const u32 e0 = blockIdx.x * kBE;
    slow_bucket((LDS char*)smem_, e0, min(kBE, E - e0), T, m, idx_in, idx_out, dist, angle, centers, freq, w_rbf, w_sbf, w_down, w_up, out);
}

__global__ void k_nop(char* ws) { if (ws == nullptr) __builtin_trap(); }
}

extern "C" void kernel_launch(void* const* d_in, const int* in_sizes, int n_in, void* d_out, int out_size, void* d_ws,
                              size_t ws_size, hipStream_t stream) {
    const u16* m = (const u16*)d_in[0];
    const int* idx_in = (const int*)d_in[1];
    const int* idx_out = (const int*)d_in[2];
    const u16* dist = (const u16*)d_in[3];
    const u16* angle = (const u16*)d_in[4];
    const u16* centers = (const u16*)d_in[5];
    const u16* freq = (const u16*)d_in[6];
    const u16* w_rbf = (const u16*)d_in[7];
    const u16* w_sbf = (const u16*)d_in[8];
    const u16* w_down = (const u16*)d_in[9];
    const u16* w_up = (const u16*)d_in[10];
    u16* out = (u16*)d_out;
    const u32 E = (u32)in_sizes[3], T = (u32)in_sizes[1];
    if (E == kE && T == kT && ws_size >= kOffEnd) {
        k_partition<<<kPB, 1024, 0, stream>>>(m, idx_in, idx_out, angle, freq, w_down, (char*)d_ws);
        k_accumulate<<<256, 1024, 0, stream>>>(m, idx_in, idx_out, dist, angle, centers, freq, w_rbf, w_sbf, w_down, w_up, out, (char*)d_ws);
        k_nop<<<256, 64, 0, stream>>>((char*)d_ws);
    } else {
        k_generic<<<(E + kBE - 1) / kBE, 1024, 0, stream>>>(E, T, m, idx_in, idx_out, dist, angle, centers, freq, w_rbf, w_sbf, w_down, w_up, out);
    }
}
```

```cpp
#include <hip/hip_runtime.h>
#include <hip/hip_bf16.h>

typedef unsigned int u32;
typedef unsigned long long u64;
typedef unsigned short u16;
typedef unsigned char u8;
typedef u32 u32x2 __attribute__((ext_vector_type(2)));
typedef u32 u32x4 __attribute__((ext_vector_type(4)));
typedef u16 u16x2 __attribute__((ext_vector_type(2)));
typedef short s16x4 __attribute__((ext_vector_type(4)));
typedef float f32x2 __attribute__((ext_vector_type(2)));
typedef float f32x4 __attribute__((ext_vector_type(4)));
typedef float f32x16 __attribute__((ext_vector_type(16)));
typedef __bf16 bf16x2 __attribute__((ext_vector_type(2)));
typedef __bf16 bf16x8 __attribute__((ext_vector_type(8)));
#define LDS __attribute__((address_space(3)))

#define LD_AUX 2
#define ST_AUX 2
#define RD_AUX 2
#define EXP_MASK 0xff8u
#define PF 4
#define DEPTH 4
#define PA1 6
#define PA2 12
#define PA3 20
#define BALC 1

namespace {

constexpr u32 kE = 320000, kT = 10240000;
constexpr u32 kNB = 256, kBE = kE / kNB;
constexpr u32 kTile = 20480, kNT = kT / kTile;
constexpr u32 kTileBytes = kTile * 5 + kNB * 3;
constexpr u32 kPB = kNT / 2, kPE = kE / kPB;
constexpr u32 kNG = 157;
constexpr u32 kThird = 1344, kRow0 = 64;

constexpr size_t kOffRec = 0;
constexpr size_t kOffDesc = (size_t)kNT * kTileBytes;
constexpr size_t kOffMax = kOffDesc + (size_t)kNT * kNB * 4;
constexpr size_t kOffFrag = kOffMax + 2048;
constexpr size_t kOffCos = kOffFrag + 2048;
constexpr size_t kOffOvf = kOffCos + 16384;
constexpr u32 kOvfWave = 32 * (kTileBytes / 4);
constexpr u32 kOvfCap = 16 * kOvfWave;
constexpr size_t kOffEnd = kOffOvf + (size_t)kOvfCap * 8;

#define STAMP(k) do {} while (0)

template <int V> struct IC { static constexpr int v = V; };
template <int B, int E, typename F>
__device__ __forceinline__ void static_for(F&& f) {
    if constexpr (B < E) { f(IC<B>{}); static_for<B + 1, E>(f); }
}
#define FENCE() __builtin_amdgcn_sched_barrier(0)

__device__ __forceinline__ float bf_lo(u32 v) { return __uint_as_float(v << 16); }
__device__ __forceinline__ float bf_hi(u32 v) { return __uint_as_float(v & 0xffff0000u); }
__device__ __forceinline__ float bf1(u16 v) { return __uint_as_float((u32)v << 16); }
__device__ __forceinline__ u32 pk_bf16(float lo, float hi) {
    f32x2 v = {lo, hi};
    return __builtin_bit_cast(u32, __builtin_convertvector(v, bf16x2));
}
__device__ __forceinline__ float rnd_bf16(float x) { return __uint_as_float(pk_bf16(0.f, x)); }
__device__ __forceinline__ auto mk_rsrc(const void* p, u32 bytes) {
    return __builtin_amdgcn_make_buffer_rsrc((void*)p, 0, bytes, 0x00020000);
}
__device__ __forceinline__ u32 pk_max16(u32 a, u32 b) {
    return __builtin_bit_cast(u32, __builtin_elementwise_max(__builtin_bit_cast(u16x2, a), __builtin_bit_cast(u16x2, b)));
}
__device__ __forceinline__ u32 pk_subsat16(u32 a, u32 b) {
    return __builtin_bit_cast(u32, __builtin_elementwise_sub_sat(__builtin_bit_cast(u16x2, a), __builtin_bit_cast(u16x2, b)));
}
__device__ __forceinline__ u32 lds_add(LDS char* p, u32 v) {
    return __hip_atomic_fetch_add((LDS u32*)p, v, __ATOMIC_RELAXED, __HIP_MEMORY_SCOPE_WORKGROUP);
}

struct AngWin { u32 ebase; bool ok; };
constexpr u32 kAngSpan = 2048;
__device__ __forceinline__ AngWin angle_window(u32 f0, u32 f1, u32 f2) {
    const u32 fm = max(f0 & 0x7fffu, max(f1 & 0x7fffu, f2 & 0x7fffu));
    const int ef = (int)((fm + 127u) >> 7) - 127;
    AngWin w;
    w.ok = ef <= 50;
    w.ebase = (u32)(121 - min(max(ef, -50), 50));
    return w;
}

struct Raw { u32x4 in, out; u32x2 an; };

__device__ __forceinline__ Raw load_chunk(const int* idx_in, const int* idx_out, const u16* angle, u32 first, u32 tid) {
    Raw r;
    r.out = __builtin_amdgcn_raw_buffer_load_b128(mk_rsrc(idx_out + first, 16384), tid * 16, 0, LD_AUX);
    r.in = __builtin_amdgcn_raw_buffer_load_b128(mk_rsrc(idx_in + first, 16384), tid * 16, 0, LD_AUX);
    r.an = __builtin_amdgcn_raw_buffer_load_b64(mk_rsrc(angle + first, 8192), tid * 8, 0, LD_AUX);
    FENCE();
    return r;
}

__device__ __forceinline__ void pack_hist(const Raw& r, u32* pay, u32* meta, u32 base2, u32& amax, LDS char* cnt) {
    const u32 c01 = pk_subsat16(r.an.x & 0x7fff7fffu, base2);
    const u32 c23 = pk_subsat16(r.an.y & 0x7fff7fffu, base2);
    amax = pk_max16(amax, pk_max16(c01, c23));
#pragma unroll
    for (int q = 0; q < 4; q++) {
        const u32 o = r.out[q], i = r.in[q];
        const u32 j = __umulhi(o, 3435974u);
        const u32 row16 = __umul24(i >> 17, kThird * 16) + ((o << 4) + kRow0 * 16 - __umul24(j, kBE * 16));
        const u32 c = q < 2 ? c01 : c23;
        const u32 ac = (q & 1) ? c & 0xffff0000u : c << 16;
        pay[q] = (__builtin_amdgcn_alignbit(i, i, 5) & 0xf800000fu) | ac | row16;
        meta[q] = ((i << 7) & 0xff0000u) | (j << 2);
        asm volatile("" : "+v"(pay[q]), "+v"(meta[q]));
        lds_add(cnt + (j << 2), 1u);
    }
    FENCE();
}

__device__ __forceinline__ void scan_tile(LDS char* cnt, LDS u32* total, u32 flag, u32* desc, u32 lane) {
    const u32x4 c = *(LDS u32x4*)(cnt + lane * 16);
    const u32x4 sz = c + ((c + 3u) >> 2);
    const u32 s1 = sz.x, s2 = s1 + sz.y, s3 = s2 + sz.z, tot = s3 + sz.w;
    u32 inc = tot;
#pragma unroll
    for (int d = 1; d < 64; d <<= 1) {
        const u32 v = __shfl_up(inc, d);
        if (lane >= (u32)d) inc += v;
    }
    const u32 ex = inc - tot;
    const u32x4 st = {ex, ex + s1, ex + s2, ex + s3};
    *(LDS u32x4*)(cnt + lane * 16) = ((st + sz - c) << 17) | (st << 2);
    if (lane == 63) *total = inc * 4u;
    *(u32x4*)(desc + lane * 4) = st | (c << 16) | flag;
}

template <int N>
__device__ __forceinline__ void scatter(const u32* pay, const u32* meta, LDS char* cnt, LDS char* stage) {
    u32 x[N];
#pragma unroll
    for (int k = 0; k < N; k++) x[k] = lds_add(cnt + (meta[k] & 0xffffu), 0x20001u);
#pragma unroll
    for (int k = 0; k < N; k++) {
        *(LDS u32*)(stage + ((x[k] >> 15) & 0x1fffcu)) = pay[k];
        *(LDS u8*)(stage + (x[k] & 0x1ffffu)) = (u8)(meta[k] >> 16);
    }
    FENCE();
}

__device__ __forceinline__ void write_tile(LDS char* stage, LDS u32* total, char* dst, u32 tid) {
    u32x4 v[7];
    const u32 tail = (__builtin_amdgcn_readfirstlane(*total) - 6 * 16384 + 127u) & ~127u;
#pragma unroll
    for (int k = 0; k < 7; k++) v[k] = *(LDS u32x4*)(stage + (k < 6 ? tid : min(tid, (kTileBytes - 6 * 16384) / 16 - 1)) * 16 + k * 16384);
#pragma unroll
    for (int k = 0; k < 7; k++)
        __builtin_amdgcn_raw_buffer_store_b128(v[k], mk_rsrc(dst + k * 16384, k < 6 ? 16384 : tail), tid * 16, 0, ST_AUX);
    FENCE();
}

__global__ void __launch_bounds__(1024) k_partition(const u16* __restrict__ m, const int* __restrict__ idx_in,
                                                    const int* __restrict__ idx_out, const u16* __restrict__ angle,
                                                    const u16* __restrict__ freq, const u16* __restrict__ w_rbf,
                                                    const u16* __restrict__ w_sbf, const u16* __restrict__ w_down, char* ws) {
    __shared__ __attribute__((aligned(16))) char smem_[2560 + kTileBytes];
    LDS char* cnt0 = (LDS char*)smem_;
    LDS char* cnt1 = cnt0 + 1024;
    LDS u32* misc = (LDS u32*)(cnt1 + 1024);
    LDS char* stage = cnt0 + 2560;
    const u32 tid = threadIdx.x, lane = tid & 63, b = blockIdx.x;
    if (b == kPB) {
        const AngWin aw = angle_window(freq[0], freq[1], freq[2]);
        u32x4 ct;
#pragma unroll 1
        for (int q = 0; q < 2; q++) {
            const float av = bf_lo((aw.ebase << 7) + tid * 2 + q);
            const u32 lo = pk_bf16(cosf(rnd_bf16(bf1(freq[0]) * av)), cosf(rnd_bf16(bf1(freq[1]) * av)));
            const u32 hi = pk_bf16(0.f, cosf(rnd_bf16(bf1(freq[2]) * av)));
            if (q) { ct.z = lo; ct.w = hi; } else { ct.x = lo; ct.y = hi; }
        }
        *(u32x4*)(ws + kOffCos + tid * 16) = ct;
        if (tid < 64) {
            const u32 row = lane & 31, vv = (row & 3) + 4 * (row >> 3);
            const bool live = ((row >> 2) & 1) == (lane >> 5);
            u32 w[12], wmax = 0, wmin = 0xffffu;
#pragma unroll
            for (int r = 0; r < 8; r++) w[r] = vv < 12 ? w_sbf[r * 12 + vv] : w_rbf[r * 4 + (vv & 3)];
#pragma unroll
            for (int r = 0; r < 4; r++) w[8 + r] = w_down[r * 4 + (lane & 3)];
#pragma unroll
            for (int r = 0; r < 12; r++) {
                wmax = max(wmax, w[r] & 0x7fffu);
                wmin = min(wmin, (w[r] & 0x7fffu) - 1u);
            }
            const u32 bad = !aw.ok || __any(wmax >= (157u << 7) || wmin < (67u << 7)) ? 1u : 0u;
            u32x4 fa = {w[0] | w[1] << 16, w[2] | w[3] << 16, w[4] | w[5] << 16, w[6] | w[7] << 16};
            if (!live) fa = u32x4{0, 0, 0, 0};
            const u32x4 fb = {w[8] | w[9] << 16, w[10] | w[11] << 16, bad, 0};
            *(u32x4*)(ws + kOffFrag + lane * 32) = fa;
            *(u32x4*)(ws + kOffFrag + lane * 32 + 16) = fb;
        }
        return;
    }
    const u32 t0 = 2 * b * kTile;
    STAMP(0);
    const u32 f01 = *(const u32*)freq, f2 = freq[2];
    const u32x4 wda = *(const u32x4*)w_down, wdb = *(const u32x4*)(w_down + 8);
    const u32x4 mv = __builtin_amdgcn_raw_buffer_load_b128(mk_rsrc(m + (size_t)b * kPE * 4, kPE * 8), tid * 16, 0, 0);
    FENCE();
    Raw ra = load_chunk(idx_in, idx_out, angle, t0, tid), rb;
    if (tid < 640) *(LDS u32*)(cnt0 + tid * 4) = 0;
    __syncthreads();
    rb = load_chunk(idx_in, idx_out, angle, t0 + 4096, tid);
    const u32 base2 = (angle_window(f01 & 0xffffu, f01 >> 16, f2).ebase << 7) * 0x10001u;
    if (tid < kPE / 2) {
        float wd[16];
#pragma unroll
        for (int k = 0; k < 4; k++) {
            wd[2 * k] = bf_lo(wda[k]); wd[2 * k + 1] = bf_hi(wda[k]);
            wd[8 + 2 * k] = bf_lo(wdb[k]); wd[9 + 2 * k] = bf_hi(wdb[k]);
        }
        u32x4 o;
#pragma unroll
        for (int h = 0; h < 2; h++) {
            const float a0 = bf_lo(mv[2 * h]), a1 = bf_hi(mv[2 * h]), a2 = bf_lo(mv[2 * h + 1]), a3 = bf_hi(mv[2 * h + 1]);
            float r[4];
#pragma unroll
            for (int d = 0; d < 4; d++) r[d] = fmaf(a3, wd[12 + d], fmaf(a2, wd[8 + d], fmaf(a1, wd[4 + d], a0 * wd[d])));
            o[2 * h] = pk_bf16(r[0], r[1]);
            o[2 * h + 1] = pk_bf16(r[2], r[3]);
        }
        u32 mx0 = pk_max16(o.x & 0x7fff7fffu, o.z & 0x7fff7fffu);
        u32 mx1 = pk_max16(o.y & 0x7fff7fffu, o.w & 0x7fff7fffu);
#pragma unroll
        for (int d = 1; d < 64; d <<= 1) {
            mx0 = pk_max16(mx0, __shfl_xor(mx0, d));
            mx1 = pk_max16(mx1, __shfl_xor(mx1, d));
        }
        if (lane == 0) { misc[2 + (tid >> 6) * 2] = mx0; misc[3 + (tid >> 6) * 2] = mx1; }
    }
    FENCE();

    u32 p0[20], m0[20], p1[20], m1[20], amax = 0;
    pack_hist(ra, p0, m0, base2, amax, cnt0);
    ra = load_chunk(idx_in, idx_out, angle, t0 + 2 * 4096, tid);
    pack_hist(rb, p0 + 4, m0 + 4, base2, amax, cnt0);
    rb = load_chunk(idx_in, idx_out, angle, t0 + 3 * 4096, tid);
    pack_hist(ra, p0 + 8, m0 + 8, base2, amax, cnt0);
    ra = load_chunk(idx_in, idx_out, angle, t0 + 4 * 4096, tid);
    pack_hist(rb, p0 + 12, m0 + 12, base2, amax, cnt0);
    rb = load_chunk(idx_in, idx_out, angle, t0 + 5 * 4096, tid);
    pack_hist(ra, p0 + 16, m0 + 16, base2, amax, cnt0);
    if (max(amax & 0xffffu, amax >> 16) >= kAngSpan) misc[0] = 1;
    __syncthreads();
    STAMP(1);
    u32* desc = (u32*)(ws + kOffDesc);
    if (tid < 64) scan_tile(cnt0, misc + 1, misc[0] << 15, desc + (2 * b) * kNB, lane);
    if (tid == 64) {
        u32 mx0 = 0, mx1 = 0;
#pragma unroll
        for (int w = 0; w < 10; w++) { mx0 = pk_max16(mx0, misc[2 + w * 2]); mx1 = pk_max16(mx1, misc[3 + w * 2]); }
        *(u32x2*)(ws + kOffMax + b * 8) = u32x2{mx0, mx1};
    }
    __syncthreads();
    FENCE();
    ra = load_chunk(idx_in, idx_out, angle, t0 + 6 * 4096, tid);
    scatter<7>(p0, m0, cnt0, stage);
    pack_hist(rb, p1, m1, base2, amax, cnt1);
    rb = load_chunk(idx_in, idx_out, angle, t0 + 7 * 4096, tid);
    scatter<7>(p0 + 7, m0 + 7, cnt0, stage);
    pack_hist(ra, p1 + 4, m1 + 4, base2, amax, cnt1);
    ra = load_chunk(idx_in, idx_out, angle, t0 + 8 * 4096, tid);
    scatter<6>(p0 + 14, m0 + 14, cnt0, stage);
    pack_hist(rb, p1 + 8, m1 + 8, base2, amax, cnt1);
    rb = load_chunk(idx_in, idx_out, angle, t0 + 9 * 4096, tid);
    pack_hist(ra, p1 + 12, m1 + 12, base2, amax, cnt1);
    __syncthreads();
    STAMP(2);
    write_tile(stage, misc + 1, ws + kOffRec + (size_t)(2 * b) * kTileBytes, tid);
    STAMP(3);
    pack_hist(rb, p1 + 16, m1 + 16, base2, amax, cnt1);
    if (max(amax & 0xffffu, amax >> 16) >= kAngSpan) misc[0] = 1;
    __syncthreads();
    STAMP(4);
    if (tid < 64) scan_tile(cnt1, misc + 1, misc[0] << 15, desc + (2 * b + 1) * kNB, lane);
    __syncthreads();
    scatter<10>(p1, m1, cnt1, stage);
    scatter<10>(p1 + 10, m1 + 10, cnt1, stage);
    __syncthreads();
    STAMP(5);
    write_tile(stage, misc + 1, ws + kOffRec + (size_t)(2 * b + 1) * kTileBytes, tid);
    STAMP(6);
}

__device__ __forceinline__ u32x4 rbf_row(float dv, u32x4 cen) {
    float r[8];
#pragma unroll
    for (int k = 0; k < 8; k++) {
        const float d = rnd_bf16(dv - ((k & 1) ? bf_hi(cen[k >> 1]) : bf_lo(cen[k >> 1])));
        const float t = rnd_bf16(-10.0f * rnd_bf16(d * d));
        r[k] = __builtin_amdgcn_exp2f(t * 1.44269504088896340736f);
    }
    return u32x4{pk_bf16(r[0], r[1]), pk_bf16(r[2], r[3]), pk_bf16(r[4], r[5]), pk_bf16(r[6], r[7])};
}

__device__ __forceinline__ u32x2 finish_edge(const float (&agg)[4], u32x2 mv, u32x2 hv, u32x4 wa, u32x4 wb) {
    float a[4], o[4];
#pragma unroll
    for (int k = 0; k < 4; k++) a[k] = rnd_bf16(agg[k]);
    const float mm[4] = {bf_lo(mv.x), bf_hi(mv.x), bf_lo(mv.y), bf_hi(mv.y)};
    const float hh[4] = {bf_lo(hv.x), bf_hi(hv.x), bf_lo(hv.y), bf_hi(hv.y)};
#pragma unroll
    for (int d = 0; d < 4; d++) {
        auto w = [&](u32 v) { return (d & 1) ? bf_hi(v) : bf_lo(v); };
        const float u = fmaf(a[3], w(wb[2 + (d >> 1)]), fmaf(a[2], w(wb[d >> 1]), fmaf(a[1], w(wa[2 + (d >> 1)]), a[0] * w(wa[d >> 1]))));
        o[d] = mm[d] + rnd_bf16(rnd_bf16(u) * hh[d]);
    }
    return u32x2{pk_bf16(o[0], o[1]), pk_bf16(o[2], o[3])};
}

__device__ __forceinline__ void slow_bucket(LDS char* sm, u32 e0, u32 ne, u32 T, const u16* m, const int* idx_in, const int* idx_out,
                                            const u16* dist, const u16* angle, const u16* centers, const u16* freq,
                                            const u16* w_rbf, const u16* w_sbf, const u16* w_down, const u16* w_up, u16* out) {
    LDS float* agg = (LDS float*)sm;
    LDS float* A = agg + kBE * 4;
    const u32 tid = threadIdx.x;
    for (u32 e = tid; e < ne; e += 1024) {
        const u32x4 rv = rbf_row(bf1(dist[e0 + e]), *(const u32x4*)centers);
#pragma unroll 1
        for (int v = 0; v < 12; v++) {
            float a = 0.f;
#pragma unroll
            for (int r = 0; r < 8; r++) a = fmaf((r & 1) ? bf_hi(rv[r >> 1]) : bf_lo(rv[r >> 1]), bf1(w_sbf[r * 12 + v]), a);
            A[e * 12 + v] = a;
        }
#pragma unroll
        for (int d = 0; d < 4; d++) agg[e * 4 + d] = 0.f;
    }
    __syncthreads();
    for (u32 t = tid; t < T; t += 1024) {
        const u32 e = (u32)idx_out[t] - e0;
        if (e < ne) {
            const u32x2 mv = *(const u32x2*)(m + (size_t)idx_in[t] * 4);
            const float av = bf1(angle[t]);
            float g[4] = {0.f, 0.f, 0.f, 0.f};
#pragma unroll 1
            for (int l = 0; l < 3; l++) {
                const float c = rnd_bf16(cosf(rnd_bf16(bf1(freq[l]) * av)));
#pragma unroll
                for (int d = 0; d < 4; d++) g[d] = fmaf(c, A[e * 12 + l * 4 + d], g[d]);
            }
#pragma unroll
            for (int d = 0; d < 4; d++) {
                const float md = fmaf(bf_hi(mv.y), bf1(w_down[12 + d]), fmaf(bf_lo(mv.y), bf1(w_down[8 + d]),
                                 fmaf(bf_hi(mv.x), bf1(w_down[4 + d]), bf_lo(mv.x) * bf1(w_down[d]))));
                __hip_atomic_fetch_add(agg + e * 4 + d, md * g[d], __ATOMIC_RELAXED, __HIP_MEMORY_SCOPE_WORKGROUP);
            }
        }
    }
    __syncthreads();
    for (u32 e = tid; e < ne; e += 1024) {
        const u32x4 rv = rbf_row(bf1(dist[e0 + e]), *(const u32x4*)centers);
        float h[4], a[4];
#pragma unroll
        for (int d = 0; d < 4; d++) {
            float s = 0.f;
#pragma unroll
            for (int r = 0; r < 8; r++) s = fmaf((r & 1) ? bf_hi(rv[r >> 1]) : bf_lo(rv[r >> 1]), bf1(w_rbf[r * 4 + d]), s);
            h[d] = s;
            a[d] = agg[e * 4 + d];
        }
        const u32x2 hv = {pk_bf16(h[0], h[1]), pk_bf16(h[2], h[3])};
        *(u32x2*)(out + (size_t)(e0 + e) * 4) =
            finish_edge(a, *(const u32x2*)(m + (size_t)(e0 + e) * 4), hv, *(const u32x4*)w_up, *(const u32x4*)(w_up + 8));
    }
}

constexpr u32 kBinOff = 2560;
constexpr u32 kMiscOff = kBinOff + kNG * 1024;
constexpr u32 kAggHalf = 3 * kThird * 8;
constexpr u32 kRbfOff = 2 * kAggHalf;
constexpr u32 kCosOff = 0x18000;
constexpr u32 kLstCap = 2560, kLstPay = 86016, kLstWin = kCosOff + 16384;
constexpr float kMagic = 12582912.0f;
constexpr u64 kUnbias = 0ull - 0x4B4000004B400000ull;

struct Consts { u32 ka, kb; bf16x8 w; s16x4 wd; };

template <int TH>
__device__ __forceinline__ u32x4 read_rbf(u32 o16, LDS char* sm) { return *(LDS u32x4*)(sm + o16 + (kRbfOff - TH * kThird * 16)); }
__device__ __forceinline__ f32x16 radial(u32x4 rb, const Consts& c) {
    const f32x16 z = {0, 0, 0, 0, 0, 0, 0, 0, 0, 0, 0, 0, 0, 0, 0, 0};
    return __builtin_amdgcn_mfma_f32_32x32x16_bf16(c.w, __builtin_bit_cast(bf16x8, rb), z, 0, 0, 0);
}
__device__ __forceinline__ f32x4 down(u32x2 mv, const Consts& c) {
    const f32x4 z = {0, 0, 0, 0};
    return __builtin_amdgcn_mfma_f32_4x4x4bf16_1k(c.wd, __builtin_bit_cast(s16x4, mv), z, 0, 0, 0);
}
__device__ __forceinline__ u32x2 read_cos(u32 p, const Consts& c, LDS char* sm) {
    return *(LDS u32x2*)(sm + __builtin_amdgcn_bitop3_b32(p >> 13, c.ka, c.kb, 0xea));
}
__device__ __forceinline__ u32x4 product(const f32x16& A, const f32x4& D, float c0, float c1, float c2) {
    u32x4 x;
#pragma unroll
    for (int d = 0; d < 4; d++)
        x[d] = __float_as_uint(fmaf(D[d], fmaf(c2, A[8 + d], fmaf(c1, A[4 + d], c0 * A[d])), kMagic));
    return x;
}
__device__ __forceinline__ void deposit(u32x4 x, u32 o16, LDS char* sm) {
    const u32x2 xa = {x.x, x.y}, xb = {x.z, x.w};
    LDS char* ap = sm + (o16 >> 1);
    __hip_atomic_fetch_add((LDS u64*)ap, __builtin_bit_cast(u64, xa) + kUnbias, __ATOMIC_RELAXED, __HIP_MEMORY_SCOPE_WORKGROUP);
    __hip_atomic_fetch_add((LDS u64*)(ap + kAggHalf), __builtin_bit_cast(u64, xb) + kUnbias, __ATOMIC_RELAXED, __HIP_MEMORY_SCOPE_WORKGROUP);
}
__device__ __forceinline__ u32 row16(u32 p) {
    u32 o = p & 0xfff0u;
    asm("" : "+v"(o));
    return o;
}
__device__ __forceinline__ u32 in8(u32 p) { return __builtin_amdgcn_alignbit(p, p, 24) & EXP_MASK; }

template <typename R>
__device__ __forceinline__ void spill(u64 ov, u32 pay, u32 wbyte, R rov, u32& nov) {
    const u32 pos = __builtin_amdgcn_mbcnt_hi((u32)(ov >> 32), __builtin_amdgcn_mbcnt_lo((u32)ov, nov));
    if (__builtin_amdgcn_inverse_ballot_w64(ov)) __builtin_amdgcn_raw_buffer_store_b64(u32x2{pay, wbyte}, rov, pos * 8, 0, 0);
    nov += __builtin_popcountll(ov);
}

struct Pair { u32x2 p; u16 w; };
struct BinK { u32 c2, c16, c1023, cdiv; };

#define BIN_ADDR(W, T, M, X, Y, B) \
    "v_mul_hi_u32_u24_sdwa %[" T "], %[cdiv], %[" X "] dst_sel:DWORD dst_unused:UNUSED_PAD src0_sel:DWORD src1_sel:WORD_0\n\t" \
    "v_lshlrev_b32_sdwa %[" W "], %[c2], %[" Y "] dst_sel:DWORD dst_unused:UNUSED_PAD src0_sel:DWORD src1_sel:" B "\n\t" \
    "v_cmp_ne_u32 %[" M "], 0, %[" X "]\n\t" \
    "v_lshl_or_b32 %[" W "], %[" T "], 10, %[" W "]\n\t"
#define BIN_CLAIM(W, T, M) \
    "s_mov_b64 exec, %[" M "]\n\t" \
    "ds_add_rtn_u32 %[" T "], %[" W "], %[c16]\n\t" \
    "v_lshl_or_b32 %[" W "], %[" W "], 6, %[c1023]\n\t"
#define BIN_STORE(W, T, M, X) \
    "s_mov_b64 exec, %[" M "]\n\t" \
    "v_cmp_le_u32 vcc, %[" T "], %[" W "]\n\t" \
    "s_mov_b64 exec, vcc\n\t" \
    "ds_write_b32 %[" T "], %[" X "] offset:%[off]\n\t" \
    "s_andn2_b64 %[" M "], %[" M "], vcc\n\t"

__device__ __forceinline__ void bin_quad(const Pair& u, const Pair& v, const BinK& k, u64 (&ov)[4]) {
    u32 w0, w1, w2, w3, t0, t1, t2, t3;
    asm volatile(
        BIN_ADDR("w0", "t0", "m0", "x0", "y0", "BYTE_0") BIN_ADDR("w1", "t1", "m1", "x1", "y0", "BYTE_1")
        BIN_ADDR("w2", "t2", "m2", "x2", "y1", "BYTE_0") BIN_ADDR("w3", "t3", "m3", "x3", "y1", "BYTE_1")
        BIN_CLAIM("w0", "t0", "m0") BIN_CLAIM("w1", "t1", "m1") BIN_CLAIM("w2", "t2", "m2") BIN_CLAIM("w3", "t3", "m3")
        "s_waitcnt lgkmcnt(0)\n\t"
        BIN_STORE("w0", "t0", "m0", "x0") BIN_STORE("w1", "t1", "m1", "x1") BIN_STORE("w2", "t2", "m2", "x2") BIN_STORE("w3", "t3", "m3", "x3")
        "s_mov_b64 exec, -1"
        : [w0] "=&v"(w0), [w1] "=&v"(w1), [w2] "=&v"(w2), [w3] "=&v"(w3), [t0] "=&v"(t0), [t1] "=&v"(t1), [t2] "=&v"(t2), [t3] "=&v"(t3),
          [m0] "=&s"(ov[0]), [m1] "=&s"(ov[1]), [m2] "=&s"(ov[2]), [m3] "=&s"(ov[3])
        : [x0] "v"(u.p.x), [x1] "v"(u.p.y), [y0] "v"(u.w), [x2] "v"(v.p.x), [x3] "v"(v.p.y), [y1] "v"(v.w),
          [c2] "v"(k.c2), [c16] "v"(k.c16), [c1023] "v"(k.c1023), [cdiv] "v"(k.cdiv), [off] "i"(kBinOff)
        : "vcc", "scc", "memory");
}
template <typename R>
__device__ __forceinline__ void spill_quad(const Pair& u, const Pair& v, const u64 (&ov)[4], R rov, u32& nov) {
    if (ov[0] | ov[1] | ov[2] | ov[3]) {
        spill(ov[0], u.p.x, u.w, rov, nov);
        spill(ov[1], u.p.y, u.w >> 8, rov, nov);
        spill(ov[2], v.p.x, v.w, rov, nov);
        spill(ov[3], v.p.y, v.w >> 8, rov, nov);
    }
}

template <int SH, typename W>
__device__ __forceinline__ void append(u32& flags, u32 pay, W w, u32 kpay, u32& pos) {
    u32 t, a, n;
    asm volatile(
        "v_add_co_u32 %[f], vcc, %[f], %[f]\n\t"
        "s_cbranch_vccz .Lapp%=\n\t"
        "s_mov_b64 exec, vcc\n\t"
        "s_bcnt1_i32_b64 %[n], vcc\n\t"
        "v_mbcnt_lo_u32_b32 %[t], vcc_lo, 0\n\t"
        "v_mbcnt_hi_u32_b32 %[t], vcc_hi, %[t]\n\t"
        "v_add_u32 %[t], %[pos], %[t]\n\t"
        "v_lshl_add_u32 %[a], %[t], 2, %[k]\n\t"
        "ds_write_b32 %[a], %[p]\n\t"
        "v_lshrrev_b32 %[a], %[sh], %[w]\n\t"
        "ds_write_b8 %[t], %[a]\n\t"
        "s_add_u32 %[pos], %[pos], %[n]\n\t"
        "s_mov_b64 exec, -1\n"
        ".Lapp%=:"
        : [f] "+v"(flags), [pos] "+s"(pos), [t] "=&v"(t), [a] "=&v"(a), [n] "=&s"(n)
        : [p] "v"(pay), [w] "v"(w), [k] "v"(kpay), [sh] "i"(SH)
        : "vcc", "scc", "memory");
}

__device__ __forceinline__ void drain_rows(LDS char* sm, u32 pair, float (&acc)[2][4], bool clear) {
#pragma unroll
    for (int k = 0; k < 3; k++) {
#pragma unroll
        for (int h = 0; h < 2; h++) {
            LDS u32x4* p = (LDS u32x4*)(sm + h * kAggHalf + (k * kThird + kRow0) * 8 + pair * 16);
            const u32x4 v = *p;
            acc[0][2 * h] += (float)(int)v.x;
            acc[0][2 * h + 1] += (float)(int)(v.y + (v.x >> 31));
            acc[1][2 * h] += (float)(int)v.z;
            acc[1][2 * h + 1] += (float)(int)(v.w + (v.z >> 31));
            if (clear) *p = u32x4{0, 0, 0, 0};
        }
    }
}

__global__ void __launch_bounds__(1024) k_accumulate(const u16* __restrict__ m, const int* __restrict__ idx_in,
                                                     const int* __restrict__ idx_out, const u16* __restrict__ dist,
                                                     const u16* __restrict__ angle, const u16* __restrict__ centers,
                                                     const u16* __restrict__ freq, const u16* __restrict__ w_rbf,
                                                     const u16* __restrict__ w_sbf, const u16* __restrict__ w_down,
                                                     const u16* __restrict__ w_up, u16* __restrict__ out, char* ws) {
    __shared__ __attribute__((aligned(16))) char smem_[163840];
    LDS char* sm = (LDS char*)smem_;
    LDS u32* misc = (LDS u32*)(sm + kMiscOff);
    const u32 tid = threadIdx.x, lane = tid & 63;
    const u32 wave = __builtin_amdgcn_readfirstlane(tid >> 6);
    const u32 j = (blockIdx.x & 7) * 32 + (blockIdx.x >> 3);
    const u32 e0 = j * kBE;
    STAMP(7);

    const u32 dsc = __builtin_amdgcn_raw_buffer_load_b32(mk_rsrc(ws + kOffDesc, kNT * kNB * 4), ((lane * 16 + wave) * kNB + j) * 4, 0, 0);
    FENCE();
    const bool own = tid < kBE / 2;
    const u32 dd = __builtin_amdgcn_raw_buffer_load_b32(mk_rsrc(dist + e0, kBE * 2), tid * 4, 0, 0);
    const u32x4 fra = *(const u32x4*)(ws + kOffFrag + lane * 32), frb = *(const u32x4*)(ws + kOffFrag + lane * 32 + 16);
    auto rmx = mk_rsrc(ws + kOffMax, wave == 15 ? kPB * 8 : 0);
    const u32x4 mxa = __builtin_amdgcn_raw_buffer_load_b128(rmx, lane * 32, 0, 0);
    const u32x4 mxb = __builtin_amdgcn_raw_buffer_load_b128(rmx, lane * 32 + 16, 0, 0);
    const u32x4 cen = *(const u32x4*)centers;
    FENCE();
#pragma unroll
    for (int k = 0; k < 10; k++) {
        const u32 s = lane * 16 + (k >> 2) * kThird * 16;
        *(LDS u32x4*)(sm + kBinOff + min(wave + k * 16, kNG - 1) * 1024 + lane * 16) = u32x4{s, s, s, s};
    }
    if (tid < kNG * 4) *(LDS u32*)(sm + tid * 4) = (tid >> 2) * 1024 + (tid & 3) * 4;
    if (tid == 1023) misc[36] = 0;

    const char* recs = ws + kOffRec;
    constexpr int D = DEPTH, NBUF = 12;
    Pair buf[NBUF];
    const u32 sbeg = min(lane * 16 + wave, kNT - 1) * kTileBytes + (dsc & 0x7fffu) * 4;
    const u32 smid = sbeg + (((dsc >> 16) + 3u) & ~3u);
    const u32 send = smid + (dsc >> 16) * 4;
    auto load_pair = [&](u32 beg, u32 mid, u32 end) {
        Pair v;
        auto rs = mk_rsrc(recs, end);
        v.w = __builtin_amdgcn_raw_buffer_load_b16(rs, lane * 2, beg, RD_AUX);
        v.p = __builtin_amdgcn_raw_buffer_load_b64(rs, lane * 8, mid, RD_AUX);
        return v;
    };
    auto issue = [&](int k) {
        buf[k % NBUF] = load_pair(__builtin_amdgcn_readlane(sbeg, k), __builtin_amdgcn_readlane(smid, k), __builtin_amdgcn_readlane(send, k));
    };
#pragma unroll
    for (int k = 0; k < 4; k++) issue(k);
    FENCE();
    __syncthreads();
    STAMP(8);

    Consts cs;
    cs.w = __builtin_bit_cast(bf16x8, fra);
    u32x4 rbs[2];
    u32x2 hv[2];
    float inv[2];
    if (wave < 10) {
#pragma unroll
        for (int q = 0; q < 2; q++) {
            u32x4 rv = rbf_row(q ? bf_hi(dd) : bf_lo(dd), cen);
            const bool bad = ((rv.x | rv.y | rv.z | rv.w) & 0x40004000u) != 0;
            if (bad) rv = u32x4{0, 0, 0, 0};
            const f32x16 A = radial(rv, cs);
            float G = 0.f;
#pragma unroll
            for (int d = 0; d < 4; d++) G = fmaxf(G, fabsf(A[d]) + fabsf(A[4 + d]) + fabsf(A[8 + d]));
            const u32 eg = max((__float_as_uint(G * 1.015625f) >> 23) + 1u, 84u);
            const float sc = __uint_as_float((271u - eg) << 23);
            inv[q] = __uint_as_float((eg - 17u) << 23);
#pragma unroll
            for (int r = 0; r < 4; r++) rbs[q][r] = pk_bf16(bf_lo(rv[r]) * sc, bf_hi(rv[r]) * sc);
            hv[q].x = bad ? 0x7fc07fc0u : pk_bf16(A[12], A[13]);
            hv[q].y = bad ? 0x7fc07fc0u : pk_bf16(A[14], A[15]);
            asm volatile("" : "+v"(rbs[q]), "+v"(hv[q]), "+v"(inv[q]));
        }
    } else if (wave == 15) {
        u32 mx0 = pk_max16(pk_max16(mxa.x, mxa.z), pk_max16(mxb.x, mxb.z));
        u32 mx1 = pk_max16(pk_max16(mxa.y, mxa.w), pk_max16(mxb.y, mxb.w));
#pragma unroll
        for (int d = 1; d < 64; d <<= 1) {
            mx0 = pk_max16(mx0, __shfl_xor(mx0, d));
            mx1 = pk_max16(mx1, __shfl_xor(mx1, d));
        }
        const u32 mb = ((lane & 2) ? mx1 : mx0) >> ((lane & 1) * 16) & 0xffffu;
        if (lane < 4) misc[32 + lane] = mb ? (mb + 127u) >> 7 : 127u;
    }
    u32 nov0 = dsc & 0x7fffu;
#pragma unroll
    for (int d = 1; d < 32; d <<= 1) nov0 += __shfl_xor(nov0, d);
    nov0 = __builtin_amdgcn_readfirstlane(nov0) + wave * kOvfWave;
    auto rov = mk_rsrc(ws + kOffOvf, kOvfCap * 8);
    u32 nov = nov0, nlate = 0, late = 0;

    BinK bk = {2, 16, 1023, 0x30c31};
    asm volatile("" : "+v"(bk.c2), "+v"(bk.c16), "+v"(bk.c1023), "+v"(bk.cdiv));
#pragma unroll
    for (int k = 4; k < D; k++) issue(k);
#pragma unroll
    for (int k = 0; k < 32; k += 2) {
        if (k == 0) __builtin_amdgcn_s_setprio(3);
        if (k == PA1) __builtin_amdgcn_s_setprio(2);
        if (k == PA2) __builtin_amdgcn_s_setprio(1);
        if (k == PA3) __builtin_amdgcn_s_setprio(0);
        if (k + D < 32) { issue(k + D); issue(k + D + 1); }
        u64 ov[4];
        bin_quad(buf[k % NBUF], buf[(k + 1) % NBUF], bk, ov);
        if (k < 32 - NBUF) {
            spill_quad(buf[k % NBUF], buf[(k + 1) % NBUF], ov, rov, nov);
        } else {
            asm volatile("v_addc_co_u32 %0, vcc, %0, %0, %1\n\tv_addc_co_u32 %0, vcc, %0, %0, %2\n\t"
                         "v_addc_co_u32 %0, vcc, %0, %0, %3\n\tv_addc_co_u32 %0, vcc, %0, %0, %4"
                         : "+v"(late) : "s"(ov[0]), "s"(ov[1]), "s"(ov[2]), "s"(ov[3]) : "vcc");
            nlate += __builtin_popcountll(ov[0]) + __builtin_popcountll(ov[1]) + __builtin_popcountll(ov[2]) + __builtin_popcountll(ov[3]);
        }
    }
    const u32x4 ctab = *(const u32x4*)(ws + kOffCos + tid * 16);
    if (__any((dsc >> 16) > 128u)) {
#pragma unroll 1
        for (int k = 0; k < 32; k++) {
            const u32 beg = __builtin_amdgcn_readlane(sbeg, k), mid = __builtin_amdgcn_readlane(smid, k), end = __builtin_amdgcn_readlane(send, k);
#pragma unroll 1
            for (u32 c = 128; mid + c * 4 < end; c += 256) {
                const Pair u = load_pair(beg + c, mid + c * 4, end), v = load_pair(beg + c + 128, mid + c * 4 + 512, end);
                u64 ov[4];
                bin_quad(u, v, bk, ov);
                spill_quad(u, v, ov, rov, nov);
            }
        }
    }
    const u32 nearly = nov - nov0, ntake = min(nearly, 64u);
    const u32x2 early = __builtin_amdgcn_raw_buffer_load_b64(mk_rsrc(ws + kOffOvf, nov * 8), lane * 8, (nov - ntake) * 8, 0);
    asm volatile("s_waitcnt lgkmcnt(0)" ::: "memory");
    if (lane == 0) {
        misc[wave] = nlate + ntake;
        misc[16 + wave] = nlate + nearly;
    }
    if (dsc & 0x8000u) misc[36] = 1;
    __syncthreads();
    STAMP(9);
    const u32 cn = misc[lane & 15], call = misc[16 + (lane & 15)];
    const u32 mex = misc[32 + (lane & 3)];
    const u32 aflag = misc[36];
    u32x4 P[10];
    auto read_bins = [&](int k) { P[k] = *(LDS u32x4*)(sm + kBinOff + min(wave + k * 16, kNG - 1) * 1024 + lane * 16); };
    if (__any(mex - 97u > 60u || (aflag | frb.z) != 0)) {
        slow_bucket(sm, e0, kBE, kT, m, idx_in, idx_out, dist, angle, centers, freq, w_rbf, w_sbf, w_down, w_up, out);
        return;
    }
#pragma unroll
    for (int k = 0; k < 7; k++) read_bins(k);
    auto rm = mk_rsrc(m, kE * 8);
    u32x2 X[6];
    auto pay = [&](auto s) {
        if constexpr (s.v < 36) return P[s.v >> 2][s.v & 3];
        else return X[s.v - 36].x;
    };
    auto gather = [&](auto s) {
        if constexpr (s.v >= 36) return __builtin_amdgcn_raw_buffer_load_b64(rm, X[s.v - 36].y, 0, 0);
        else return __builtin_amdgcn_raw_buffer_load_b64(rm, in8(pay(s)), ((wave + (s.v >> 2) * 16) * 4 + (s.v & 3)) * 4096, 0);
    };
    u32x2 md[PF];
    md[0] = gather(IC<0>{});
    FENCE();
    u32 inc = cn;
    static_for<0, 4>([&](auto d) { inc += __builtin_amdgcn_update_dpp(0u, inc, 0x110 + (1 << d.v), 0xf, 0xf, true); });
    const bool fits = inc <= kLstCap;
    const u32 nfit = __builtin_popcount((u32)__builtin_amdgcn_ballot_w64(fits) & 0xffffu);
    const u32 nlist = nfit ? __builtin_amdgcn_readlane(inc, nfit - 1) : 0u;
    const u32 rest = fits ? call - cn : call;
    const bool more = __any(rest != 0);
    __syncthreads();
    md[1] = gather(IC<1>{});
    FENCE();
    {
        const u32x4 z = {0, 0, 0, 0};
#pragma unroll
        for (int k = 0; k < 4; k++) *(LDS u32x4*)(sm + (tid + k * 1024) * 16) = z;
        FENCE();
        md[2] = gather(IC<2>{});
        FENCE();
        if (own) {
            *(LDS u32x4*)(sm + kRbfOff + kRow0 * 16 + tid * 32) = rbs[0];
            *(LDS u32x4*)(sm + kRbfOff + kRow0 * 16 + tid * 32 + 16) = rbs[1];
        }
        *(LDS u32x4*)(sm + kCosOff + tid * 16) = ctab;
        const float sc = __uint_as_float((254u - mex) << 23);
        cs.wd = __builtin_bit_cast(s16x4, u32x2{pk_bf16(bf_lo(frb.x) * sc, bf_hi(frb.x) * sc), pk_bf16(bf_lo(frb.y) * sc, bf_hi(frb.y) * sc)});
        cs.ka = 0x3ff8u;
        cs.kb = kCosOff;
        asm volatile("" : "+v"(cs.ka), "+v"(cs.kb));
    }
    float mp[4];
#pragma unroll
    for (int d = 0; d < 4; d++) mp[d] = __uint_as_float(__builtin_amdgcn_readlane(mex, d) << 23);
    if (wave < nfit) {
        u32 pos = kLstWin + __builtin_amdgcn_readlane(inc - cn, wave);
        u32 fl = (late << (32 - 2 * NBUF)) | (lane < ntake ? 1u << (31 - 2 * NBUF) : 0u);
        const u32 kpay = kLstPay - kLstWin * 4;
#pragma unroll
        for (int k = 32 - NBUF; k < 32; k++) {
            const Pair v = buf[k % NBUF];
            append<0>(fl, v.p.x, v.w, kpay, pos);
            append<8>(fl, v.p.y, v.w, kpay, pos);
        }
        append<0>(fl, early.x, early.y, kpay, pos);
        asm volatile("s_waitcnt lgkmcnt(0)" ::: "memory");
        nov -= ntake;
    } else {
#pragma unroll
        for (int k = 32 - NBUF; k < 32; k += 2) {
            u64 ov[4];
#pragma unroll
            for (int q = 0; q < 4; q++) ov[q] = __builtin_amdgcn_ballot_w64((late & (1u << (2 * (31 - k) + 1 - q))) != 0);
            spill_quad(buf[k % NBUF], buf[(k + 1) % NBUF], ov, rov, nov);
        }
    }
    FENCE();
    md[3] = gather(IC<3>{});
    FENCE();
    auto fetch_listed = [&](u32 slot) {
        const u32 i = min(slot * 64 + lane, kLstCap - 1);
        u32x2 r = {*(LDS u32*)(sm + kLstPay + i * 4), *(LDS u8*)(sm + kLstWin + i)};
        if (slot * 64 + lane >= nlist) r.x = 0;
        return r;
    };
    auto fix_spilled = [&](u32x2& r) {
        if (r.x == 0) r = u32x2{lane * 16, 0};
        const u32 th = __umulhi(r.x & 0xfff0u, 0x30c31u);
        r.y = in8(r.x) + (((r.y & 255u) | (th << 8)) << 12);
        r.x += (2u - th) * (kThird * 16);
    };
    __syncthreads();
    STAMP(10);
    u32x4 mv;
    {
        u32 o0 = row16(pay(IC<0>{})), o1 = row16(pay(IC<1>{})), o2 = 0;
        u32x4 rb = read_rbf<0>(o0, sm);
        float c0, c1, c2;
        u32x2 ct = read_cos(pay(IC<0>{}), cs, sm);
        static_for<4, PF>([&](auto s) { md[s.v] = gather(s); });
        f32x16 A = radial(rb, cs);
        f32x4 Dn = down(md[0], cs);
        rb = read_rbf<0>(o1, sm);
        c0 = bf_lo(ct.x); c1 = bf_hi(ct.x); c2 = __uint_as_float(ct.y);
        ct = read_cos(pay(IC<1>{}), cs, sm);
        md[0] = gather(IC<PF>{});
        FENCE();
        auto step = [&](auto sc) {
            constexpr int s = sc.v;
            if constexpr (BALC == 1 && s % 11 == 0) __builtin_amdgcn_s_setprio(3 - s / 11);
            asm volatile("" :: "v"(A));
            const u32x4 x = product(A, Dn, c0, c1, c2);
            FENCE();
            if constexpr (s + 1 < 42) {
                A = radial(rb, cs);
                Dn = down(md[(s + 1) % PF], cs);
                c0 = bf_lo(ct.x); c1 = bf_hi(ct.x); c2 = __uint_as_float(ct.y);
                FENCE();
            }
            if constexpr (s + 2 < 42) {
                o2 = row16(pay(IC<s + 2>{}));
                rb = read_rbf<((s + 2) >> 4)>(o2, sm);
                ct = read_cos(pay(IC<s + 2>{}), cs, sm);
            }
            FENCE();
            deposit(x, o0, sm);
            if constexpr (s + 1 + PF < 42) md[(s + 1) % PF] = gather(IC<s + 1 + PF>{});
            FENCE();
            o0 = o1; o1 = o2;
            FENCE();
        };
        static_for<0, 16>(step);
        read_bins(7);
        read_bins(8);
        FENCE();
        static_for<16, 22>(step);
        if (wave + 144 < kNG) {
            read_bins(9);
#pragma unroll
            for (int q = 0; q < 4; q++) X[q] = u32x2{P[9][q], (wave + 16) * 4 + q};
        } else {
#pragma unroll
            for (int q = 0; q < 4; q++) X[q] = fetch_listed((wave - 13) * 4 + q);
        }
        X[4] = fetch_listed(12 + wave);
        X[5] = fetch_listed(28 + wave);
#pragma unroll
        for (int q = 0; q < 6; q++) fix_spilled(X[q]);
        mv = __builtin_amdgcn_raw_buffer_load_b128(mk_rsrc(m + (size_t)e0 * 4, kBE * 8), tid * 16, 0, 0);
        FENCE();
        static_for<22, 41>(step);
        if ((28 + wave) * 64 < nlist) step(IC<41>{});
    }
    STAMP(11);
    const u32x4 wua = *(const u32x4*)w_up, wub = *(const u32x4*)(w_up + 8);
    float acc[2][4] = {{0.f, 0.f, 0.f, 0.f}, {0.f, 0.f, 0.f, 0.f}};
    if (more) {
        u32 it = 10;
#pragma unroll 1
        for (u32 i0 = 0; __any(i0 < rest); i0 += 64) {
            if (++it == 16) {
                it = 0;
                __syncthreads();
                if (own) drain_rows(sm, tid, acc, true);
                __syncthreads();
            }
            if (nov0 + i0 < nov) {
                u32x2 r = __builtin_amdgcn_raw_buffer_load_b64(mk_rsrc(ws + kOffOvf, nov * 8), lane * 8, (nov0 + i0) * 8, 0);
                fix_spilled(r);
                const u32x2 g = __builtin_amdgcn_raw_buffer_load_b64(rm, r.y, 0, 0);
                const u32x2 ct = read_cos(r.x, cs, sm);
                const u32 o = row16(r.x);
                deposit(product(radial(read_rbf<2>(o, sm), cs), down(g, cs), bf_lo(ct.x), bf_hi(ct.x), __uint_as_float(ct.y)), o, sm);
            }
        }
    }
    __syncthreads();
    STAMP(12);
    if (own) {
        drain_rows(sm, tid, acc, false);
        u32x4 o;
#pragma unroll
        for (int q = 0; q < 2; q++) {
            float a[4];
#pragma unroll
            for (int d = 0; d < 4; d++) a[d] = acc[q][d] * inv[q] * mp[d];
            const u32x2 r = finish_edge(a, u32x2{mv[2 * q], mv[2 * q + 1]}, hv[q], wua, wub);
            o[2 * q] = r.x;
            o[2 * q + 1] = r.y;
        }
        *(u32x4*)(out + ((size_t)e0 + tid * 2) * 4) = o;
    }
    STAMP(13);
}

__global__ void __launch_bounds__(1024) k_generic(u32 E, u32 T, const u16* __restrict__ m, const int* __restrict__ idx_in,
                                                  const int* __restrict__ idx_out, const u16* __restrict__ dist,
                                                  const u16* __restrict__ angle, const u16* __restrict__ centers,
                                                  const u16* __restrict__ freq, const u16* __restrict__ w_rbf,
                                                  const u16* __restrict__ w_sbf, const u16* __restrict__ w_down,
                                                  const u16* __restrict__ w_up, u16* __restrict__ out) {
    __shared__ __attribute__((aligned(16))) char smem_[kBE * 64];
    const u32 e0 = blockIdx.x * kBE;
    slow_bucket((LDS char*)smem_, e0, min(kBE, E - e0), T, m, idx_in, idx_out, dist, angle, centers, freq, w_rbf, w_sbf, w_down, w_up, out);
}

}

extern "C" void kernel_launch(void* const* d_in, const int* in_sizes, int n_in, void* d_out, int out_size, void* d_ws,
                              size_t ws_size, hipStream_t stream) {
    const u16* m = (const u16*)d_in[0];
    const int* idx_in = (const int*)d_in[1];
    const int* idx_out = (const int*)d_in[2];
    const u16* dist = (const u16*)d_in[3];
    const u16* angle = (const u16*)d_in[4];
    const u16* centers = (const u16*)d_in[5];
    const u16* freq = (const u16*)d_in[6];
    const u16* w_rbf = (const u16*)d_in[7];
    const u16* w_sbf = (const u16*)d_in[8];
    const u16* w_down = (const u16*)d_in[9];
    const u16* w_up = (const u16*)d_in[10];
    u16* out = (u16*)d_out;
    const u32 E = (u32)in_sizes[3], T = (u32)in_sizes[1];
    if (E == kE && T == kT && ws_size >= kOffEnd) {
        k_partition<<<kPB + 1, 1024, 0, stream>>>(m, idx_in, idx_out, angle, freq, w_rbf, w_sbf, w_down, (char*)d_ws);
        k_accumulate<<<256, 1024, 0, stream>>>(m, idx_in, idx_out, dist, angle, centers, freq, w_rbf, w_sbf, w_down, w_up, out, (char*)d_ws);
    } else {
        k_generic<<<(E + kBE - 1) / kBE, 1024, 0, stream>>>(E, T, m, idx_in, idx_out, dist, angle, centers, freq, w_rbf, w_sbf, w_down, w_up, out);
    }
}
```

```cpp
#include <hip/hip_runtime.h>
#include <hip/hip_bf16.h>

typedef unsigned int u32;
typedef unsigned long long u64;
typedef unsigned short u16;
typedef unsigned char u8;
typedef u32 u32x2 __attribute__((ext_vector_type(2)));
typedef u32 u32x3 __attribute__((ext_vector_type(3)));
typedef u32 u32x4 __attribute__((ext_vector_type(4)));
typedef u16 u16x2 __attribute__((ext_vector_type(2)));
typedef short s16x4 __attribute__((ext_vector_type(4)));
typedef float f32x2 __attribute__((ext_vector_type(2)));
typedef float f32x4 __attribute__((ext_vector_type(4)));
typedef float f32x16 __attribute__((ext_vector_type(16)));
typedef __bf16 bf16x2 __attribute__((ext_vector_type(2)));
typedef __bf16 bf16x8 __attribute__((ext_vector_type(8)));
#define LDS __attribute__((address_space(3)))

extern "C" __device__ u32x3 raw_load_x3(u32x4 srd, u32 voff, u32 soff, int aux) __asm("llvm.amdgcn.raw.buffer.load.v3i32");

namespace {

constexpr u32 kE = 320000, kT = 10240000;
constexpr u32 kNB = 256, kBE = kE / kNB;
constexpr u32 kPairE = 2 * kBE, kHalf = kE / 2;
constexpr u32 kTile = 20480, kNT = kT / kTile;
constexpr u32 kTileBytes = kTile * 5 + kNB * 7;
constexpr u32 kPB = kNT / 2, kPE = kE / kPB;
constexpr u32 kNG = 157;
constexpr u32 kThird = 2520, kRow0 = 16;
constexpr u32 kThirdDiv = 213045;

constexpr u32 kLeader = kPB;
constexpr u32 kSpinCap = 1u << 15;
constexpr u32 kSpread = 960;
constexpr int kSc1 = 16, kNt = 2;

constexpr size_t kOffRec = 0;
constexpr size_t kOffDesc = (size_t)kNT * kTileBytes;
constexpr size_t kOffMax = kOffDesc + (size_t)kNT * kNB * 4;
constexpr size_t kOffHand = kOffMax + 4096;
constexpr size_t kOffXch = kOffHand + kNB * 128;
constexpr u32 kXchBytes = 20480;
constexpr size_t kOffOvf = kOffXch + kNB * kXchBytes;
constexpr u32 kOvfWave = 32 * 128;
constexpr size_t kOffEnd = kOffOvf + (size_t)kNB * 16 * kOvfWave * 8;

template <int V> struct IC { static constexpr int v = V; };
template <int B, int E, typename F>
__device__ __forceinline__ void static_for(F&& f) {
    if constexpr (B < E) { f(IC<B>{}); static_for<B + 1, E>(f); }
}
#define FENCE() __builtin_amdgcn_sched_barrier(0)

__device__ __forceinline__ float bf_lo(u32 v) { return __uint_as_float(v << 16); }
__device__ __forceinline__ float bf_hi(u32 v) { return __uint_as_float(v & 0xffff0000u); }
__device__ __forceinline__ float bf1(u16 v) { return __uint_as_float((u32)v << 16); }
__device__ __forceinline__ u32 pk_bf16(float lo, float hi) {
    f32x2 v = {lo, hi};
    return __builtin_bit_cast(u32, __builtin_convertvector(v, bf16x2));
}
__device__ __forceinline__ float rnd_bf16(float x) { return __uint_as_float(pk_bf16(0.f, x)); }
__device__ __forceinline__ auto mk_rsrc(const void* p, u32 bytes) {
    return __builtin_amdgcn_make_buffer_rsrc((void*)p, 0, bytes, 0x00020000);
}
__device__ __forceinline__ u32 pk_max16(u32 a, u32 b) {
    return __builtin_bit_cast(u32, __builtin_elementwise_max(__builtin_bit_cast(u16x2, a), __builtin_bit_cast(u16x2, b)));
}
__device__ __forceinline__ u32 pk_min16(u32 a, u32 b) {
    return __builtin_bit_cast(u32, __builtin_elementwise_min(__builtin_bit_cast(u16x2, a), __builtin_bit_cast(u16x2, b)));
}
__device__ __forceinline__ u32 pk_subsat16(u32 a, u32 b) {
    return __builtin_bit_cast(u32, __builtin_elementwise_sub_sat(__builtin_bit_cast(u16x2, a), __builtin_bit_cast(u16x2, b)));
}
__device__ __forceinline__ u32 lds_add(LDS char* p, u32 v) {
    return __hip_atomic_fetch_add((LDS u32*)p, v, __ATOMIC_RELAXED, __HIP_MEMORY_SCOPE_WORKGROUP);
}

struct AngWin { u32 ebase; bool ok; };
constexpr u32 kAngSpan = 2048;
__device__ __forceinline__ AngWin angle_window(u32 f0, u32 f1, u32 f2) {
    const u32 fm = max(f0 & 0x7fffu, max(f1 & 0x7fffu, f2 & 0x7fffu));
    const int ef = (int)((fm + 127u) >> 7) - 127;
    AngWin w;
    w.ok = ef <= 50;
    w.ebase = (u32)(121 - min(max(ef, -50), 50));
    return w;
}


struct Raw { u32x4 in, out; u32x2 an; };

__device__ __forceinline__ Raw load_chunk(const int* idx_in, const int* idx_out, const u16* angle, u32 first, u32 tid) {
    Raw r;
    r.out = __builtin_amdgcn_raw_buffer_load_b128(mk_rsrc(idx_out + first, 16384), tid * 16, 0, kNt);
    r.in = __builtin_amdgcn_raw_buffer_load_b128(mk_rsrc(idx_in + first, 16384), tid * 16, 0, kNt);
    r.an = __builtin_amdgcn_raw_buffer_load_b64(mk_rsrc(angle + first, 8192), tid * 8, 0, kNt);
    FENCE();
    return r;
}

__device__ __forceinline__ void pack_hist(const Raw& r, u32* pay, u32* meta, u32 base2, u32& amax, LDS char* cnt) {
    u32 rank[4], pair[4], rel[4];
#pragma unroll
    for (int q = 0; q < 4; q++) {
        const u32 t = r.in[q] - kHalf;
        rel[q] = min(r.in[q], t);
        pair[q] = __umulhi(r.out[q], 1717987u);
        meta[q] = ((t >> 24) & 0x80u) | pair[q];
        rank[q] = lds_add(cnt + (meta[q] << 2), 1u);
    }
    const u32 c01 = pk_subsat16(r.an.x & 0x7fff7fffu, base2);
    const u32 c23 = pk_subsat16(r.an.y & 0x7fff7fffu, base2);
    amax = pk_max16(amax, pk_max16(c01, c23));
#pragma unroll
    for (int q = 0; q < 4; q++) {
        const u32 i = rel[q];
        const u32 row = __umul24(i >> 16, kThird * 8) + ((r.out[q] << 3) + kRow0 * 8 - __umul24(pair[q], kPairE * 8));
        const u32 c = q < 2 ? c01 : c23;
        const u32 ac = (q & 1) ? c & 0xffff0000u : c << 16;
        pay[q] = (__builtin_amdgcn_alignbit(i, i, 5) & 0xf8000007u) | ac | row;
        meta[q] = __builtin_amdgcn_perm(meta[q], i, 0x0c0c0401u);
        asm volatile("" : "+v"(pay[q]), "+v"(meta[q]));
    }
#pragma unroll
    for (int q = 0; q < 4; q++) {
        meta[q] |= rank[q] << 16;
        asm volatile("" : "+v"(meta[q]));
    }
    FENCE();
}

__device__ __forceinline__ u32 wave_scan(u32 v) {
    static_for<0, 4>([&](auto d) { v += __builtin_amdgcn_update_dpp(0u, v, 0x110 + (1 << d.v), 0xf, 0xf, true); });
    v += __builtin_amdgcn_update_dpp(0u, v, 0x142, 0xa, 0xf, false);
    v += __builtin_amdgcn_update_dpp(0u, v, 0x143, 0xc, 0xf, false);
    return v;
}

__device__ __forceinline__ u32 wave_max16(u32 v) {
    static_for<0, 4>([&](auto d) { v = pk_max16(v, __builtin_amdgcn_update_dpp(0u, v, 0x110 + (1 << d.v), 0xf, 0xf, true)); });
    v = pk_max16(v, __builtin_amdgcn_update_dpp(0u, v, 0x142, 0xa, 0xf, false));
    return pk_max16(v, __builtin_amdgcn_update_dpp(0u, v, 0x143, 0xc, 0xf, false));
}

__device__ __forceinline__ u32 seg_words(u32 n) { return (n >> 2) * 5 + ((0x4330u >> ((n & 3) * 4)) & 7u); }

__device__ __forceinline__ u32x4 scan_tile(LDS char* cnt, LDS char* stage, LDS u32* total, u32 flag, u32 lane) {
    const u32x4 c = *(LDS u32x4*)(cnt + lane * 16);
    const u32x4 sz = {seg_words(c.x), seg_words(c.y), seg_words(c.z), seg_words(c.w)};
    const u32 s1 = sz.x, s2 = s1 + sz.y, s3 = s2 + sz.z, tot = s3 + sz.w;
    const u32 inc = wave_scan(tot);
    const u32 ex = inc - tot;
    const u32x4 st = {ex, ex + s1, ex + s2, ex + s3};
    *(LDS u32x4*)(cnt + lane * 16) = st << 2;
#pragma unroll
    for (int q = 0; q < 4; q++)
        if ((c[q] & 3u) == 1u) *(LDS u32*)(stage + (st[q] + sz[q] - 3u) * 4u) = 0;
    if (lane == 63) *total = inc * 4u;
    return st | (c << 16) | (flag | 0x80000000u);
}

template <int N>
__device__ __forceinline__ void scatter(const u32* pay, const u32* meta, LDS char* cnt, LDS char* stage) {
    u32 x[N];
#pragma unroll
    for (int k = 0; k < N; k++) x[k] = *(LDS u32*)(cnt + ((meta[k] >> 6) & 0x3fcu));
#pragma unroll
    for (int k = 0; k < N; k++) {
        const u32 r = (meta[k] >> 16) & 3u;
        const u32 q = __umul24(meta[k] >> 18, 20u) + x[k];
        *(LDS u32*)(stage + (q + __builtin_amdgcn_perm(0u, 0x100c0004u, r | 0x0c0c0c00u))) = pay[k];
        *(LDS u8*)(stage + (q + r) + 8) = (u8)meta[k];
    }
    FENCE();
}

struct Staged { u32x4 v[7]; u32 tail; };
__device__ __forceinline__ void read_tile(Staged& t, LDS char* stage, LDS u32* total, u32 tid) {
    t.tail = (__builtin_amdgcn_readfirstlane(*total) - 6 * 16384 + 127u) & ~127u;
#pragma unroll
    for (int k = 0; k < 7; k++) t.v[k] = *(LDS u32x4*)(stage + (k < 6 ? tid : min(tid, (kTileBytes - 6 * 16384) / 16 - 1)) * 16 + k * 16384);
    FENCE();
}
__device__ __forceinline__ void store_piece(const Staged& t, int k, char* dst, u32 tid) {
    __builtin_amdgcn_raw_buffer_store_b128(t.v[k], mk_rsrc(dst + k * 16384, k < 6 ? 16384 : t.tail), tid * 16, 0, kSc1);
    FENCE();
}


__device__ __forceinline__ u32x4 rbf_row(float dv, u32x4 cen) {
    float r[8];
#pragma unroll
    for (int k = 0; k < 8; k++) {
        const float d = rnd_bf16(dv - ((k & 1) ? bf_hi(cen[k >> 1]) : bf_lo(cen[k >> 1])));
        const float t = rnd_bf16(-10.0f * rnd_bf16(d * d));
        r[k] = __builtin_amdgcn_exp2f(t * 1.44269504088896340736f);
    }
    return u32x4{pk_bf16(r[0], r[1]), pk_bf16(r[2], r[3]), pk_bf16(r[4], r[5]), pk_bf16(r[6], r[7])};
}

__device__ __forceinline__ u32x2 finish_edge(const float (&agg)[4], u32x2 mv, u32x2 hv, u32x4 wa, u32x4 wb) {
    float a[4], o[4];
#pragma unroll
    for (int k = 0; k < 4; k++) a[k] = rnd_bf16(agg[k]);
    const float mm[4] = {bf_lo(mv.x), bf_hi(mv.x), bf_lo(mv.y), bf_hi(mv.y)};
    const float hh[4] = {bf_lo(hv.x), bf_hi(hv.x), bf_lo(hv.y), bf_hi(hv.y)};
#pragma unroll
    for (int d = 0; d < 4; d++) {
        auto w = [&](u32 v) { return (d & 1) ? bf_hi(v) : bf_lo(v); };
        const float u = fmaf(a[3], w(wb[2 + (d >> 1)]), fmaf(a[2], w(wb[d >> 1]), fmaf(a[1], w(wa[2 + (d >> 1)]), a[0] * w(wa[d >> 1]))));
        o[d] = mm[d] + rnd_bf16(rnd_bf16(u) * hh[d]);
    }
    return u32x2{pk_bf16(o[0], o[1]), pk_bf16(o[2], o[3])};
}

__device__ __forceinline__ void slow_bucket(LDS char* sm, u32 e0, u32 ne, u32 T, const u16* m, const int* idx_in, const int* idx_out,
                                            const u16* dist, const u16* angle, const u16* centers, const u16* freq,
                                            const u16* w_rbf, const u16* w_sbf, const u16* w_down, const u16* w_up, u16* out) {
    LDS float* agg = (LDS float*)sm;
    LDS float* A = agg + kBE * 4;
    const u32 tid = threadIdx.x;
    for (u32 e = tid; e < ne; e += 1024) {
        const u32x4 rv = rbf_row(bf1(dist[e0 + e]), *(const u32x4*)centers);
#pragma unroll 1
        for (int v = 0; v < 12; v++) {
            float a = 0.f;
#pragma unroll
            for (int r = 0; r < 8; r++) a = fmaf((r & 1) ? bf_hi(rv[r >> 1]) : bf_lo(rv[r >> 1]), bf1(w_sbf[r * 12 + v]), a);
            A[e * 12 + v] = a;
        }
#pragma unroll
        for (int d = 0; d < 4; d++) agg[e * 4 + d] = 0.f;
    }
    __syncthreads();
    for (u32 t = tid; t < T; t += 1024) {
        const u32 e = (u32)idx_out[t] - e0;
        if (e < ne) {
            const u32x2 mv = *(const u32x2*)(m + (size_t)idx_in[t] * 4);
            const float av = bf1(angle[t]);
            float g[4] = {0.f, 0.f, 0.f, 0.f};
#pragma unroll 1
            for (int l = 0; l < 3; l++) {
                const float c = rnd_bf16(cosf(rnd_bf16(bf1(freq[l]) * av)));
#pragma unroll
                for (int d = 0; d < 4; d++) g[d] = fmaf(c, A[e * 12 + l * 4 + d], g[d]);
            }
#pragma unroll
            for (int d = 0; d < 4; d++) {
                const float md = fmaf(bf_hi(mv.y), bf1(w_down[12 + d]), fmaf(bf_lo(mv.y), bf1(w_down[8 + d]),
                                 fmaf(bf_hi(mv.x), bf1(w_down[4 + d]), bf_lo(mv.x) * bf1(w_down[d]))));
                __hip_atomic_fetch_add(agg + e * 4 + d, md * g[d], __ATOMIC_RELAXED, __HIP_MEMORY_SCOPE_WORKGROUP);
            }
        }
    }
    __syncthreads();
    for (u32 e = tid; e < ne; e += 1024) {
        const u32x4 rv = rbf_row(bf1(dist[e0 + e]), *(const u32x4*)centers);
        float h[4], a[4];
#pragma unroll
        for (int d = 0; d < 4; d++) {
            float s = 0.f;
#pragma unroll
            for (int r = 0; r < 8; r++) s = fmaf((r & 1) ? bf_hi(rv[r >> 1]) : bf_lo(rv[r >> 1]), bf1(w_rbf[r * 4 + d]), s);
            h[d] = s;
            a[d] = agg[e * 4 + d];
        }
        const u32x2 hv = {pk_bf16(h[0], h[1]), pk_bf16(h[2], h[3])};
        *(u32x2*)(out + (size_t)(e0 + e) * 4) =
            finish_edge(a, *(const u32x2*)(m + (size_t)(e0 + e) * 4), hv, *(const u32x4*)w_up, *(const u32x4*)(w_up + 8));
    }
}


constexpr u32 kBinOff = 2560;
constexpr u32 kMiscOff = kBinOff + kNG * 1024;
constexpr u32 kParOff = kMiscOff + 160;
constexpr u32 kPlane = kThird * 8;
constexpr u32 kAggOff = kPlane;
constexpr u32 kRbfOff = kAggOff + 4 * kPlane;
constexpr u32 kLstCap = 2752, kLstOff = kRbfOff + 2 * kPlane;
constexpr int kSwitch = 21;
constexpr int kSlots = 42, kAhead = 5, kBufs = 12;
constexpr u32 kFail = 36, kOdd = 37, kCol = 32;
constexpr u32 kNone = 0xf0000000u;
constexpr float kMagic = 12582912.0f;
constexpr u64 kUnbias = 0ull - 0x4B4000004B400000ull;

struct Consts { u32 rbf0; bf16x8 w; s16x4 wd; };

template <int TH>
__device__ __forceinline__ u32x4 read_rbf(u32 o8, const Consts& c, LDS char* sm) {
    if constexpr (TH == 0) return *(LDS u32x4*)(sm + ((o8 << 1) + c.rbf0));
    else return *(LDS u32x4*)(sm + (o8 + o8) + (kRbfOff - TH * 2 * kPlane));
}
__device__ __forceinline__ f32x16 radial(u32x4 rb, const Consts& c) {
    const f32x16 z = {0, 0, 0, 0, 0, 0, 0, 0, 0, 0, 0, 0, 0, 0, 0, 0};
    return __builtin_amdgcn_mfma_f32_32x32x16_bf16(c.w, __builtin_bit_cast(bf16x8, rb), z, 0, 0, 0);
}
__device__ __forceinline__ f32x4 down(u32x2 mv, const Consts& c) {
    const f32x4 z = {0, 0, 0, 0};
    return __builtin_amdgcn_mfma_f32_4x4x4bf16_1k(c.wd, __builtin_bit_cast(s16x4, mv), z, 0, 0, 0);
}
__device__ __forceinline__ u32x2 read_cos(u32 p, LDS char* sm) { return *(LDS u32x2*)(sm + ((p >> 13) & 0x3ff8u)); }
__device__ __forceinline__ u32x4 product(const f32x16& A, const f32x4& D, float c0, float c1, float c2) {
    u32x4 x;
#pragma unroll
    for (int d = 0; d < 4; d++)
        x[d] = __float_as_uint(fmaf(D[d], fmaf(c2, A[8 + d], fmaf(c1, A[4 + d], c0 * A[d])), kMagic));
    return x;
}
template <int TH, int BUF>
__device__ __forceinline__ void deposit(u32x4 x, u32 o8, LDS char* sm) {
    const u32x2 xa = {x.x, x.y}, xb = {x.z, x.w};
    LDS char* ap = sm + o8 + (kAggOff + (2 * BUF - TH) * (int)kPlane);
    __hip_atomic_fetch_add((LDS u64*)ap, __builtin_bit_cast(u64, xa) + kUnbias, __ATOMIC_RELAXED, __HIP_MEMORY_SCOPE_WORKGROUP);
    __hip_atomic_fetch_add((LDS u64*)(ap + kPlane), __builtin_bit_cast(u64, xb) + kUnbias, __ATOMIC_RELAXED, __HIP_MEMORY_SCOPE_WORKGROUP);
}
__device__ __forceinline__ u32 row8(u32 p) {
    u32 o = p & 0xfff8u;
    asm("" : "+v"(o));
    return o;
}
__device__ __forceinline__ u32 in8(u32 p) { return __builtin_amdgcn_alignbit(p, p, 24) & 0x7f8u; }

template <typename R>
__device__ __forceinline__ void spill(u64 ov, u32 pay, u32 wbyte, R rov, u32& nov) {
    const u32 pos = __builtin_amdgcn_mbcnt_hi((u32)(ov >> 32), __builtin_amdgcn_mbcnt_lo((u32)ov, nov));
    if (__builtin_amdgcn_inverse_ballot_w64(ov)) {
        __builtin_amdgcn_raw_buffer_store_b32(pay, rov, pos * 8, 0, 0);
        asm volatile("" ::: "memory");
        __builtin_amdgcn_raw_buffer_store_b32(wbyte, rov, pos * 8 + 4, 0, 0);
    }
    nov += __builtin_popcountll(ov);
}

typedef u32x3 Pair;
struct BinK { u32 c2, c16, c1023, cdiv, selx, selz; };
__device__ __forceinline__ Pair unswizzle(Pair v, const BinK& k) {
    return Pair{__builtin_amdgcn_perm(v.x, v.z, k.selx), v.y, __builtin_amdgcn_perm(v.x, v.z, k.selz)};
}

#define BIN_CLAIM(M, N, X, Y, Z, WA, WB, TA, TB) \
    "v_cmpx_ne_u32 %[" M "], 0, %[" Y "]\n\t" \
    "v_mul_hi_u32_u24_sdwa %[" TA "], %[cdiv], %[" Y "] dst_sel:DWORD dst_unused:UNUSED_PAD src0_sel:DWORD src1_sel:WORD_0\n\t" \
    "v_lshlrev_b32_sdwa %[" WA "], %[c2], %[" Z "] dst_sel:DWORD dst_unused:UNUSED_PAD src0_sel:DWORD src1_sel:BYTE_0\n\t" \
    "v_mul_hi_u32_u24_sdwa %[" TB "], %[cdiv], %[" X "] dst_sel:DWORD dst_unused:UNUSED_PAD src0_sel:DWORD src1_sel:WORD_0\n\t" \
    "v_lshl_or_b32 %[" WA "], %[" TA "], 10, %[" WA "]\n\t" \
    "v_lshlrev_b32_sdwa %[" WB "], %[c2], %[" Z "] dst_sel:DWORD dst_unused:UNUSED_PAD src0_sel:DWORD src1_sel:BYTE_2\n\t" \
    "ds_add_rtn_u32 %[" TA "], %[" WA "], %[c16]\n\t" \
    "v_lshl_or_b32 %[" WB "], %[" TB "], 10, %[" WB "]\n\t" \
    "v_lshl_or_b32 %[" WA "], %[" WA "], 6, %[c1023]\n\t" \
    "v_cmpx_ne_u32 %[" N "], 0, %[" X "]\n\t" \
    "ds_add_rtn_u32 %[" TB "], %[" WB "], %[c16]\n\t" \
    "v_lshl_or_b32 %[" WB "], %[" WB "], 6, %[c1023]\n\t"
#define BIN_STORE(M, N, X, Y, WA, WB, TA, TB, OA, OB) \
    "s_mov_b64 exec, %[" M "]\n\t" \
    "v_cmp_gt_u32 %[" OA "], %[" TA "], %[" WA "]\n\t" \
    "v_cmp_gt_u32 %[" OB "], %[" TB "], %[" WB "]\n\t" \
    "s_andn2_b64 exec, %[" M "], %[" OA "]\n\t" \
    "ds_write_b32 %[" TA "], %[" Y "] offset:%[off]\n\t" \
    "s_andn2_b64 exec, %[" N "], %[" OB "]\n\t" \
    "ds_write_b32 %[" TB "], %[" X "] offset:%[off]\n\t"

__device__ __forceinline__ void bin_quad(const Pair& u, const Pair& v, const BinK& k, u64 (&ov)[4]) {
    u32 w0, w1, w2, w3, t0, t1, t2, t3;
    u64 ma, mb, na, nb;
    asm volatile(
        BIN_CLAIM("ma", "na", "ux", "uy", "uz", "w0", "w1", "t0", "t1")
        "s_mov_b64 exec, -1\n\t"
        BIN_CLAIM("mb", "nb", "vx", "vy", "vz", "w2", "w3", "t2", "t3")
        "s_waitcnt lgkmcnt(0)\n\t"
        BIN_STORE("ma", "na", "ux", "uy", "w0", "w1", "t0", "t1", "o0", "o1")
        BIN_STORE("mb", "nb", "vx", "vy", "w2", "w3", "t2", "t3", "o2", "o3")
        "s_mov_b64 exec, -1"
        : [w0] "=&v"(w0), [w1] "=&v"(w1), [w2] "=&v"(w2), [w3] "=&v"(w3), [t0] "=&v"(t0), [t1] "=&v"(t1), [t2] "=&v"(t2), [t3] "=&v"(t3),
          [ma] "=&s"(ma), [mb] "=&s"(mb), [na] "=&s"(na), [nb] "=&s"(nb),
          [o0] "=&s"(ov[0]), [o1] "=&s"(ov[1]), [o2] "=&s"(ov[2]), [o3] "=&s"(ov[3])
        : [ux] "v"(u.x), [uy] "v"(u.y), [uz] "v"(u.z), [vx] "v"(v.x), [vy] "v"(v.y), [vz] "v"(v.z),
          [c2] "v"(k.c2), [c16] "v"(k.c16), [c1023] "v"(k.c1023), [cdiv] "v"(k.cdiv), [off] "i"(kBinOff)
        : "scc", "memory");
}
template <typename R>
__device__ __forceinline__ void spill_quad(const Pair& u, const Pair& v, const u64 (&ov)[4], R rov, u32& nov) {
    if (__builtin_expect((ov[0] | ov[1] | ov[2] | ov[3]) != 0, 0)) {
        spill(ov[0], u.y, u.z, rov, nov);
        spill(ov[1], u.x, u.z >> 16, rov, nov);
        spill(ov[2], v.y, v.z, rov, nov);
        spill(ov[3], v.x, v.z >> 16, rov, nov);
    }
}

__device__ __forceinline__ void mark_late(u32& late, const u64 (&ov)[4]) {
    asm volatile("v_addc_co_u32 %0, vcc, %0, %0, %1\n\tv_addc_co_u32 %0, vcc, %0, %0, %2\n\t"
                 "v_addc_co_u32 %0, vcc, %0, %0, %3\n\tv_addc_co_u32 %0, vcc, %0, %0, %4"
                 : "+v"(late) : "s"(ov[0]), "s"(ov[1]), "s"(ov[2]), "s"(ov[3]) : "vcc");
}

#define APPEND(P, SKIP, LABEL) \
    "v_add_co_u32 %[f], vcc, %[f], %[f]\n\t" SKIP \
    "s_mov_b64 exec, vcc\n\t" \
    "ds_write2_b32 %[a], %[" P "], %[w] offset1:1\n\t" \
    "v_add_u32 %[a], 8, %[a]\n\t" \
    "s_mov_b64 exec, -1\n" LABEL
template <bool SPARSE>
__device__ __forceinline__ void append2(u32& flags, const Pair& v, u32& a) {
    u32 w = v.z;
    if constexpr (SPARSE)
        asm volatile(APPEND("y", "s_cbranch_vccz .Lapa%=\n\t", ".Lapa%=:\n\t")
                     APPEND("x", "s_cbranch_vccz .Lapb%=\n\tv_lshrrev_b32 %[w], 16, %[w]\n\t", ".Lapb%=:\n\t")
                     : [f] "+v"(flags), [a] "+v"(a), [w] "+v"(w) : [x] "v"(v.x), [y] "v"(v.y) : "vcc", "memory");
    else
        asm volatile(APPEND("y", "", "") APPEND("x", "v_lshrrev_b32 %[w], 16, %[w]\n\t", "")
                     : [f] "+v"(flags), [a] "+v"(a), [w] "+v"(w) : [x] "v"(v.x), [y] "v"(v.y) : "vcc", "memory");
}

#define ZROW(o) "ds_write_addtid_b32 %[z] offset:" #o "*4096\n\t"
__device__ __forceinline__ void clear_sums(u32 wave) {
    static_assert(4 * kPlane + kRow0 * 16 == 19 * 4096 + 12 * 256, "");
    u32 keep;
    asm volatile(
        "s_mov_b32 %[keep], m0\n\ts_mov_b32 m0, %[a]\n\ts_nop 0\n\t"
        ZROW(0) ZROW(1) ZROW(2) ZROW(3) ZROW(4) ZROW(5) ZROW(6) ZROW(7) ZROW(8) ZROW(9) ZROW(10) ZROW(11) ZROW(12) ZROW(13) ZROW(14) ZROW(15)
        "s_add_u32 m0, %[a], 8*4096\n\ts_nop 0\n\t" ZROW(8) ZROW(9) ZROW(10)
        "s_mov_b32 m0, %[b]\n\ts_nop 0\n\t" ZROW(11)
        "s_mov_b32 m0, %[keep]"
        : [keep] "=&s"(keep) : [z] "v"(0u), [a] "s"(kAggOff + wave * 256), [b] "s"(kAggOff + 8 * 4096 + min(wave, 11u) * 256) : "scc", "memory");
}

__device__ __forceinline__ void drain_row(LDS char* sm, u32 row, f32x2 (&acc)[2], bool clear) {
#pragma unroll
    for (int k = 0; k < 4; k++) {
        LDS u64* p = (LDS u64*)(sm + kAggOff + k * kPlane + row * 8);
        const u64 v = *p;
        u64 t = v + 0x80000000ull;
        asm("" : "+v"(t));
        acc[k & 1] += f32x2{(float)(int)(u32)v, (float)(int)(t >> 32)};
        if (clear) *p = 0;
    }
}

__device__ __forceinline__ float cos_bf16_arg(float x) {
    return __builtin_amdgcn_cosf(fmaf(x, 1.95847275472e-06f, __builtin_amdgcn_fractf(x * 0.159152984619140625f)));
}
__device__ __forceinline__ u32 warm_code(u32 line) {
    u64 pc;
    int len;
    asm volatile("s_getpc_b64 %0\n.Lgm_pc%=:\n\ts_mov_b32 %1, .Lgm_next-.Lgm_pc%=" : "=s"(pc), "=s"(len));
    return __builtin_amdgcn_raw_buffer_load_b32(mk_rsrc((const void*)pc, max(len, 0)), line * 128, 0, 0);
}
#define LANDING(n) asm volatile(".Lgm_land" #n ":\n\ts_cbranch_execz .Lgm_back")
#define VISIT(n) "s_cmp_eq_u32 %0, " #n "\n\ts_cbranch_scc1 .Lgm_land" #n "\n\t"
__device__ __forceinline__ void visit_landings(u32 wave) {
    asm volatile("s_mov_b64 exec, 0\n\t" VISIT(1) VISIT(2) VISIT(3) VISIT(4) VISIT(5) VISIT(6) VISIT(7)
                 "\n.Lgm_back:\n\ts_mov_b64 exec, -1" :: "s"(wave) : "scc");
}
template <typename T>
__device__ __forceinline__ T* GPTR(T* p) { return (T*)(__attribute__((address_space(1))) T*)p; }
#define DRAIN() asm volatile("s_waitcnt vmcnt(0)" ::: "memory")

struct Args {
    const int *idx_in, *idx_out;
    const u16 *angle, *m;
    char* ws;
    const u16 *dist, *w_sbf, *w_rbf, *w_down, *w_up, *centers, *freq;
    u16* out;
};

__global__ void __launch_bounds__(1024, 4) k_fused(Args ar) {
    __shared__ __attribute__((aligned(16))) char smem_[163840];
    LDS char* sm = (LDS char*)smem_;
    LDS u32* misc = (LDS u32*)(sm + kMiscOff);
    LDS char* par = sm + kParOff;
    const u32 tid = threadIdx.x, lane = tid & 63, b = blockIdx.x;
    const u32 wave = __builtin_amdgcn_readfirstlane(tid >> 6) & 15u;
    const u32 side = b & 1, pairi = ((b & 7) >> 1) * 32 + (b >> 3);
    const u32 j = side * 128 + pairi;
    const u32 e0 = pairi * kPairE + side * kBE, e1 = pairi * kPairE + (side ^ 1) * kBE;
    const bool mine1 = wave < 4;
    auto loc = [&](int q) { return q == 0 ? tid : q == 2 ? tid + 256 : mine1 ? tid + 1024 : tid - 256; };
    auto lrow = [&](int q) { return kRow0 + ((q == 0 || (q == 1 && mine1)) ? side : side ^ 1) * kBE + loc(q); };
    char* const ws = ar.ws;
    const u16* const m = ar.m;
    const u32 piece = wave * kPB + ((b & 7) * 31 + min(b & 7, 2u) + (b >> 3) + wave * 31) % kPB;
    const u32 t0 = (piece - wave) * 256;
    Raw ra, rb;
    if (b < kPB) ra = load_chunk(ar.idx_in, ar.idx_out, ar.angle, t0, tid);

    const u32 cline = wave == 15 && lane < 16 ? lane * 32 + (b >> 3) : 0x100000u;
    const u32 code0 = warm_code(cline);
    const u16* psrc = wave < 2 ? ar.w_sbf + wave * 64 : wave == 2 ? ar.w_rbf : wave == 3 ? ar.w_down : wave == 4 ? ar.w_up : wave == 5 ? ar.centers : ar.freq;
    const u32 pcnt = (u32)(0x03081010202040ull >> (min(wave, 7u) * 8)) & 255u;
    const u32 pdst = (u32)(0xa8a09080604000ull >> (min(wave, 7u) * 8)) & 255u;
    const u16 pv = __builtin_amdgcn_raw_buffer_load_b16(mk_rsrc(psrc, 0x1000), lane < pcnt ? lane * 2 : kNone, 0, 0);
    auto rhs = mk_rsrc(ws + kOffHand, kNB * 128);
    const u32 hoff = tid == 0 ? b * 128 : kNone;
    const u32x2 found = __builtin_amdgcn_raw_buffer_load_b64(rhs, hoff, 0, kSc1);
    u16 dd[3];
    dd[0] = __builtin_amdgcn_raw_buffer_load_b16(mk_rsrc(ar.dist + e0, 0x1000), tid * 2, 0, 0);
    dd[1] = __builtin_amdgcn_raw_buffer_load_b16(mk_rsrc(ar.dist + (mine1 ? e0 : e1), 0x1000), min(loc(1), kBE - 1) * 2, 0, 0);
    dd[2] = __builtin_amdgcn_raw_buffer_load_b16(mk_rsrc(ar.dist + e1, wave < 8 ? 0 : 0x1000), min(loc(2), kBE - 1) * 2, 0, 0);
    FENCE();
    u32 nonce = 0, seen = 0;
    auto post_request = [&]() {
        nonce = found.x + 1u;
        if (nonce == found.y) nonce++;
        __builtin_amdgcn_raw_buffer_store_b32(nonce, rhs, hoff, 0, kSc1);
    };
    auto peek = [&](u32 word) {
        asm volatile("" ::: "memory");
        seen = __builtin_amdgcn_raw_buffer_load_b32(rhs, hoff + word * 4, 0, kSc1);
        FENCE();
    };
    auto await_word = [&](u32 word, u32 want) {
#pragma unroll 1
        for (u32 spins = 0; __builtin_amdgcn_readfirstlane(seen != want); spins++) {
            if (spins > kSpinCap) {
                misc[kFail] = 1;
                return false;
            }
            __builtin_amdgcn_s_sleep(8);
            peek(word);
        }
        asm volatile("" ::: "memory");
        return true;
    };
    auto post_ack = [&]() { __builtin_amdgcn_raw_buffer_store_b32(1u, rhs, hoff + 8, 0, kSc1); };

    Consts cs;
    u32x2 wdraw;
    u32x4 ctab;
    auto setup_frags = [&]() {
        const u32 row = lane & 31, vv = (row & 3) + 4 * (row >> 3);
        const u32 wo = vv < 12 ? vv * 2 : 192 + (vv & 3) * 2, wstep = vv < 12 ? 24 : 8;
        u32 w[12], wmax = 0, wmin = 0xffffu;
#pragma unroll
        for (int r = 0; r < 8; r++) w[r] = *(LDS u16*)(par + wo + r * wstep);
#pragma unroll
        for (int r = 0; r < 4; r++) w[8 + r] = *(LDS u16*)(par + 256 + r * 8 + (lane & 3) * 2);
        if (wave == 15) {
            const u32x2 fq = *(LDS u32x2*)(par + 336);
            u32 col = 0;
#pragma unroll
            for (int r = 0; r < 12; r++) {
                wmax = max(wmax, w[r] & 0x7fffu);
                wmin = min(wmin, (w[r] & 0x7fffu) - 1u);
                if (r == 7 && vv < 12) col = wmax;
            }
            col = max(col, (u32)__shfl_xor(col, 8));
            col = max(col, (u32)__shfl_xor(col, 16));
            u32 top = max(col, (u32)__shfl_xor(col, 1));
            top = max(top, (u32)__shfl_xor(top, 2));
            if (lane < 4) misc[kCol + lane] = col ? top - col : 0u;
            misc[kOdd] = __any(!angle_window(fq.x & 0xffffu, fq.x >> 16, fq.y).ok || wmax >= (157u << 7) || wmin < (67u << 7) ||
                               top - 1u < (107u << 7)) ? 1u : 0u;
        }
        u32x4 fa = {w[0] | w[1] << 16, w[2] | w[3] << 16, w[4] | w[5] << 16, w[6] | w[7] << 16};
        if (((row >> 2) & 1) != (lane >> 5)) fa = u32x4{0, 0, 0, 0};
        wdraw = u32x2{w[8] | w[9] << 16, w[10] | w[11] << 16};
        cs.w = __builtin_bit_cast(bf16x8, fa);
        FENCE();
    };
    auto setup_cos = [&]() {
        const u32x2 fq = *(LDS u32x2*)(par + 336);
        const u32 code = (angle_window(fq.x & 0xffffu, fq.x >> 16, fq.y).ebase << 7) + tid * 2;
#pragma unroll
        for (int q = 0; q < 2; q++) {
            const float av = bf_lo(code + q);
            ctab[2 * q] = pk_bf16(cos_bf16_arg(rnd_bf16(bf_lo(fq.x) * av)), cos_bf16_arg(rnd_bf16(bf_hi(fq.x) * av)));
            ctab[2 * q + 1] = pk_bf16(0.f, cos_bf16_arg(rnd_bf16(bf_lo(fq.y) * av)));
        }
        asm volatile("" : "+v"(ctab));
        FENCE();
    };
    u32x4 rbs[3];
    u32x3 gate0;
    auto setup_edge = [&](int q) {
        if (q < 2 || wave >= 8) {
            u32x4 rv = rbf_row(bf1(dd[q]), *(LDS u32x4*)(par + 320));
            const bool bad = ((rv.x | rv.y | rv.z | rv.w) & 0x40004000u) != 0;
            if (bad) rv = u32x4{0, 0, 0, 0};
            const f32x16 A = radial(rv, cs);
            float G = 0.f;
#pragma unroll
            for (int d = 0; d < 4; d++) G = fmaxf(G, fabsf(A[d]) + fabsf(A[4 + d]) + fabsf(A[8 + d]));
            const u32 eg = max((__float_as_uint(G * 1.34f) >> 23) + 1u, 84u);
            const float sc = __uint_as_float((271u - eg) << 23);
#pragma unroll
            for (int r = 0; r < 4; r++) rbs[q][r] = pk_bf16(bf_lo(rv[r]) * sc, bf_hi(rv[r]) * sc);
            asm volatile("" : "+v"(rbs[q]));
            if (q == 0 || (q == 1 && mine1)) {
                const u32x3 g = {bad ? 0x7fc07fc0u : pk_bf16(A[12], A[13]), bad ? 0x7fc07fc0u : pk_bf16(A[14], A[15]), (eg - 17u) << 23};
                if (q == 0) {
                    gate0 = g;
                    asm volatile("" : "+v"(gate0.x), "+v"(gate0.y), "+v"(gate0.z));
                } else {
                    rbs[2] = u32x4{g.x, g.y, g.z, 0};
                }
            }
        }
        FENCE();
    };
    auto fill_bins = [&](int k) {
        const u32 s = (lane & 15) * 8 + (k >> 2) * kPlane;
        *(LDS u32x4*)(sm + kBinOff + k * 16384 + (k < 9 ? tid : min(tid, 12 * 64 + lane)) * 16) = u32x4{s, s, s, s};
        FENCE();
    };
    auto rdesc = mk_rsrc(ws + kOffDesc, kNT * kNB * 4);
    u32x4 desc1 = {0, 0, 0, 0};
    bool echoed = false;
    u32 doff, dsc;
    const u32 pb = (lane & 15) * 16 + wave;
    const u32 tile = min(pb, kPB - 1) * 2 + ((lane >> 4) & 1);
    auto first_look = [&]() {
        doff = lane < 32 && pb < kPB && misc[kFail] == 0 ? (tile * kNB + j) * 4 : kNone;
        dsc = __builtin_amdgcn_raw_buffer_load_b32(rdesc, lane < 16 ? doff : kNone, 0, kSc1);
        FENCE();
    };

    if (__builtin_expect(b < kPB, 1)) {
        LDS char* cnt0 = sm;
        LDS char* cnt1 = sm + 1024;
        LDS u32* pm = (LDS u32*)(sm + 2048);
        LDS char* stage = sm + 2560;
        auto chunk = [&](int c) { return load_chunk(ar.idx_in, ar.idx_out, ar.angle, t0 + c * (kPB * 4096), tid); };
        const u32x4 mv = __builtin_amdgcn_raw_buffer_load_b128(mk_rsrc(m + (size_t)b * kPE * 4, kPE * 8), tid * 16, 0, 0);
        if (tid < 640) *(LDS u32*)(cnt0 + tid * 4) = 0;
        if (tid == 1023) misc[kFail] = 0;
        FENCE();
        rb = chunk(1);
        if (lane < pcnt) *(LDS u16*)(par + (pdst + lane) * 2) = pv;
        asm volatile("" :: "v"(code0));
        post_request();
        if (wave == 0) {
            const u32x4 z = {0, 0, 0, 0};
            __builtin_amdgcn_raw_buffer_store_b128(z, rdesc, (2 * b * kNB + lane * 4) * 4, 0, kSc1);
            __builtin_amdgcn_raw_buffer_store_b128(z, rdesc, ((2 * b + 1) * kNB + lane * 4) * 4, 0, kSc1);
            __builtin_amdgcn_raw_buffer_store_b32(0u, mk_rsrc(ws + kOffMax + b * 16, 16), lane * 4, 0, kSc1);
            __builtin_amdgcn_raw_buffer_store_b32(0u, rhs, hoff + 16, 0, kSc1);
        }
        __syncthreads();
        const u32x2 fq = *(LDS u32x2*)(par + 336);
        const u32 base2 = (angle_window(fq.x & 0xffffu, fq.x >> 16, fq.y).ebase << 7) * 0x10001u;
        u32 p0[20], m0[20], p1[20], m1[20], amax = 0;
        pack_hist(ra, p0, m0, base2, amax, cnt0);
        ra = chunk(2);
        pack_hist(rb, p0 + 4, m0 + 4, base2, amax, cnt0);
        setup_frags();
        setup_cos();
        rb = chunk(3);
        pack_hist(ra, p0 + 8, m0 + 8, base2, amax, cnt0);
        if (wave < 10) {
            cs.wd = __builtin_bit_cast(s16x4, wdraw);
            const f32x4 da = down(u32x2{mv.x, mv.y}, cs), db = down(u32x2{mv.z, mv.w}, cs);
            const u32x4 o = u32x4{pk_bf16(da.x, da.y), pk_bf16(da.z, da.w), pk_bf16(db.x, db.y), pk_bf16(db.z, db.w)} & 0x7fff7fffu;
            const u32x4 ex = (o >> 7) & 0x00ff00ffu;
            const u32 nz = pk_min16(o.x, 0x10001u) + pk_min16(o.z, 0x10001u) + ((pk_min16(o.y, 0x10001u) + pk_min16(o.w, 0x10001u)) << 8);
            const u32x4 t = {wave_max16(pk_max16(o.x, o.z)), wave_max16(pk_max16(o.y, o.w)), wave_scan(ex.x + ex.z), wave_scan(ex.y + ex.w)};
            const u32 n = wave_scan(nz);
            if (lane == 63) {
                *(LDS u32x4*)(pm + 8 + wave * 8) = t;
                pm[12 + wave * 8] = n;
            }
        }
        FENCE();
        setup_edge(2);
        ra = chunk(4);
        pack_hist(rb, p0 + 12, m0 + 12, base2, amax, cnt0);
        setup_edge(0);
        peek(1);
        rb = chunk(5);
        pack_hist(ra, p0 + 16, m0 + 16, base2, amax, cnt0);
        if (max(amax & 0xffffu, amax >> 16) >= kAngSpan) pm[0] = 1;
        __syncthreads();
        u32x4 desc0 = {0, 0, 0, 0};
        u32 mx = 0;
        if (wave == 0) desc0 = scan_tile(cnt0, stage, pm + 1, pm[0] << 15, lane);
        __syncthreads();
        if (wave == 0) {
            DRAIN();
            echoed = await_word(1, nonce);
            u32 s0 = 0, s1 = 0, nn = 0;
#pragma unroll
            for (int w = 0; w < 10; w++) {
                const u32 v = pm[8 + w * 8 + (lane & 3)];
                mx = pk_max16(mx, v);
                s0 += v & 0xffffu;
                s1 += v >> 16;
                nn += (pm[12 + w * 8] >> ((lane & 1) * 8)) & 0x00ff00ffu;
            }
            auto lowered = [](u32 sum, u32 cnt) { return cnt ? 0x7fffu - (u32)((float)(sum << 7) * __builtin_amdgcn_rcpf((float)cnt)) : 0u; };
            if (lane & 2) mx = lowered(s0, nn & 0xffffu) | lowered(s1, nn >> 16) << 16;
            asm volatile("" : "+v"(mx));
        }
        FENCE();
        ra = chunk(6);
        scatter<7>(p0, m0, cnt0, stage);
        pack_hist(rb, p1, m1, base2, amax, cnt1);
        rb = chunk(7);
        scatter<7>(p0 + 7, m0 + 7, cnt0, stage);
        pack_hist(ra, p1 + 4, m1 + 4, base2, amax, cnt1);
        ra = chunk(8);
        if (echoed) post_ack();
        scatter<6>(p0 + 14, m0 + 14, cnt0, stage);
        pack_hist(rb, p1 + 8, m1 + 8, base2, amax, cnt1);
        rb = chunk(9);
        pack_hist(ra, p1 + 12, m1 + 12, base2, amax, cnt1);
        __syncthreads();
        Staged st;
        char* dst = ws + kOffRec + (size_t)(2 * b) * kTileBytes;
        read_tile(st, stage, pm + 1, tid);
        pack_hist(rb, p1 + 16, m1 + 16, base2, amax, cnt1);
        if (max(amax & 0xffffu, amax >> 16) >= kAngSpan) pm[0] = 1;
        auto peek_go = [&]() {
            asm volatile("" ::: "memory");
            seen = __builtin_amdgcn_raw_buffer_load_b32(rhs, b * 128 + 12, 0, kSc1);
            FENCE();
        };
        peek_go();
        __syncthreads();
        if (wave == 0) desc1 = scan_tile(cnt1, stage, pm + 1, pm[0] << 15, lane);
        visit_landings(wave);
        __syncthreads();
        scatter<3>(p1, m1, cnt1, stage);
#pragma unroll 1
        for (u32 spins = 32; spins && __builtin_amdgcn_readfirstlane(seen) != 1u; spins--) peek_go();
        static_for<0, 7>([&](auto k) {
            constexpr int a = 3 + 2 * k.v, e = k.v == 6 ? 20 : a + 2;
            if constexpr (k.v == 2) LANDING(1);
            if constexpr (k.v == 6) LANDING(2);
            store_piece(st, k.v, dst, tid);
            scatter<e - a>(p1 + a, m1 + a, cnt1, stage);
        });
        if (echoed) await_word(3, 1u);
        DRAIN();
        __syncthreads();
        if (wave == 0) {
            __builtin_amdgcn_raw_buffer_store_b128(desc0, rdesc, (2 * b * kNB + lane * 4) * 4, 0, kSc1);
            __builtin_amdgcn_raw_buffer_store_b32(mx | 0x80008000u, mk_rsrc(ws + kOffMax + b * 16, 16), lane * 4, 0, kSc1);
        }
        FENCE();
        read_tile(st, stage, pm + 1, tid);
        dst += kTileBytes;
        store_piece(st, 0, dst, tid);
        __syncthreads();
        LANDING(3);
#pragma unroll
        for (int k = 0; k < 6; k++) {
            store_piece(st, k + 1, dst, tid);
            if (k < 5) { fill_bins(2 * k); fill_bins(2 * k + 1); }
        }
        first_look();
    } else {
        if (lane < pcnt) *(LDS u16*)(par + (pdst + lane) * 2) = pv;
        if (tid == 0) misc[kFail] = 0;
#pragma unroll
        for (int k = 0; k < 10; k++) fill_bins(k);
        __builtin_amdgcn_raw_buffer_store_b32(0u, rhs, hoff + 16, 0, kSc1);
        if (b != kLeader) {
            if (wave == 0) {
                post_request();
                DRAIN();
                peek(1);
                if (await_word(1, nonce)) {
                    post_ack();
                    peek(3);
                    await_word(3, 1u);
                }
            }
        } else {
            __builtin_amdgcn_raw_buffer_store_b64(u32x2{0, 0}, rhs, tid * 128 + 8, 0, kSc1);
            DRAIN();
            __syncthreads();
            if (wave < 4) {
                u32 last = 0;
#pragma unroll 1
                for (u32 spins = 0;; spins++) {
                    asm volatile("" ::: "memory");
                    const u32x4 h = __builtin_amdgcn_raw_buffer_load_b128(rhs, tid * 128, 0, kSc1);
                    if (spins == 0 || h.x != last) __builtin_amdgcn_raw_buffer_store_b32(h.x, rhs, tid * 128 + 4, 0, kSc1);
                    last = h.x;
                    if (__all(h.z != 0 || tid == kLeader)) break;
                    if (spins > kSpinCap) {
                        misc[kFail] = 1;
                        break;
                    }
                    __builtin_amdgcn_s_sleep(2);
                }
            }
            __syncthreads();
            if (misc[kFail] == 0) __builtin_amdgcn_raw_buffer_store_b32(1u, rhs, tid * 128 + 12, 0, kSc1);
        }
        __syncthreads();
        setup_frags();
        setup_cos();
        setup_edge(2);
        setup_edge(0);
        first_look();
    }
    if (tid < kNG * 4) *(LDS u32*)(sm + tid * 4) = (tid >> 2) * 1024 + (tid & 3) * 4;
    LANDING(4);
    setup_edge(1);

    const char* recs = ws + kOffRec;
    Pair buf[kBufs];
    u32 sbeg, send;
    u32 todo = 0xffffffffu, ready = 0, looked = 0;
    auto decode = [&]() {
        todo ^= looked ^ ready;
        if ((int)dsc < 0) doff = kNone;
        looked = ready = (u32)__builtin_amdgcn_ballot_w64(doff == kNone) & todo;
        sbeg = tile * kTileBytes + (dsc & 0x7fffu) * 4;
        send = sbeg + seg_words((dsc >> 16) & 0x7fffu) * 4;
    };
    const u32 voff = (lane >> 1) * 20 + (lane & 1) * 8;
    u32x4 srda = {(u32)(u64)recs, (u32)((u64)recs >> 32) & 0xffffu, 0, 0x00020000u}, srdb = srda;
    asm volatile("" : "+s"(srda), "+s"(srdb));
    u32 bega, begb;
    auto pick2 = [&]() {
        if (__builtin_expect((ready & (ready - 1)) == 0, 0)) {
            u32 spins = 0;
#pragma unroll 1
            do {
                if (spins) __builtin_amdgcn_s_sleep(8);
                asm volatile("" ::: "memory");
                dsc |= __builtin_amdgcn_raw_buffer_load_b32(rdesc, doff, 0, kSc1);
                if (++spins > kSpinCap) {
                    misc[kFail] = 1;
                    doff = kNone;
                }
                decode();
            } while ((ready & (ready - 1)) == 0);
        }
        u32 g0, g1;
        asm("s_ff1_i32_b32 %0, %2\n\ts_bitset0_b32 %2, %0\n\ts_ff1_i32_b32 %1, %2\n\ts_bitset0_b32 %2, %1" : "=&s"(g0), "=&s"(g1), "+s"(ready));
        srda.z = __builtin_amdgcn_readlane(send, g0);
        bega = __builtin_amdgcn_readlane(sbeg, g0);
        srdb.z = __builtin_amdgcn_readlane(send, g1);
        begb = __builtin_amdgcn_readlane(sbeg, g1);
        asm volatile("" : "+s"(srda), "+s"(srdb), "+s"(bega), "+s"(begb));
        FENCE();
    };
    auto load2 = [&](int k) {
        buf[k % kBufs] = raw_load_x3(srda, voff, bega, kSc1);
        buf[(k + 1) % kBufs] = raw_load_x3(srdb, voff, begb, kSc1);
        FENCE();
    };
    decode();
    DRAIN();
    __syncthreads();
    if (wave == 0 && b < kPB) __builtin_amdgcn_raw_buffer_store_b128(desc1, rdesc, ((2 * b + 1) * kNB + lane * 4) * 4, 0, kSc1);
    LANDING(5);
    pick2();
    load2(0);
    pick2();
    auto rov = mk_rsrc(ws + kOffOvf + (size_t)(b * 16 + wave) * (kOvfWave * 8), kOvfWave * 8);
    auto rmx = mk_rsrc(ws + kOffMax, kPB * 16);
    u32x4 mxv;
    u32 nov = 0, late = 0, dnext = 0, code1 = 0;

    BinK bk = {2, 16, 1023, kThirdDiv, (lane & 1) ? 0x03020100u : 0x07060504u, (lane & 1) ? 0x0c070c06u : 0x0c010c00u};
    asm volatile("" : "+v"(bk.c2), "+v"(bk.c16), "+v"(bk.c1023), "+v"(bk.cdiv));
    static_for<0, 16>([&](auto kc) {
        constexpr int k = 2 * kc.v;
        if constexpr (k == 2) LANDING(6);
        if constexpr (k == 6) LANDING(7);
        if (k + 2 < 32) load2(k + 2);
        if (k == 0) __builtin_amdgcn_s_setprio(3);
        if (k == 8 || k == 24) __builtin_amdgcn_s_setprio(2);
        if (k == 14 || k == 28) __builtin_amdgcn_s_setprio(1);
        if (k == 18 || k == 30) __builtin_amdgcn_s_setprio(0);
        if (k == 12 || k == 20) { dsc |= dnext; decode(); }
        if (k == 8 || k == 16) dnext = __builtin_amdgcn_raw_buffer_load_b32(rdesc, doff, 0, kSc1);
        if (k == 20) code1 = warm_code(cline);
        if (k == 26) mxv = __builtin_amdgcn_raw_buffer_load_b128(rmx, tid * 16, 0, kSc1);
        if (k + 4 < 32) pick2();
        if (k == 32 - kBufs) {
            __builtin_amdgcn_s_barrier();
            __builtin_amdgcn_s_setprio(3);
        }
        u64 ov[4];
        buf[k % kBufs] = unswizzle(buf[k % kBufs], bk);
        buf[(k + 1) % kBufs] = unswizzle(buf[(k + 1) % kBufs], bk);
        bin_quad(buf[k % kBufs], buf[(k + 1) % kBufs], bk, ov);
        if (k < 32 - kBufs) spill_quad(buf[k % kBufs], buf[(k + 1) % kBufs], ov, rov, nov);
        else mark_late(late, ov);
    });
    const u32 ntake = min(nov, 64u);
    const u32 lpos = wave_scan(__builtin_popcount(late));
    const u32 nlate = __builtin_amdgcn_readlane(lpos, 63);
    const u32x2 early = __builtin_amdgcn_raw_buffer_load_b64(rov, lane < ntake ? (nov - ntake + lane) * 8 : kNone, 0, 0);
    if (wave < 4 && misc[kFail] == 0) {
#pragma unroll 1
        for (u32 spins = 0; __any(tid < kPB && (int)(mxv.x & mxv.y & mxv.z & mxv.w) >= 0); spins++) {
            if (spins > kSpinCap) {
                misc[kFail] = 1;
                break;
            }
            __builtin_amdgcn_s_sleep(8);
            asm volatile("" ::: "memory");
            mxv = __builtin_amdgcn_raw_buffer_load_b128(rmx, tid * 16, 0, kSc1);
        }
#pragma unroll
        for (int q = 0; q < 4; q++) mxv[q] = wave_max16(mxv[q]);
        if (lane == 63) *(LDS u32x4*)(par + wave * 16) = mxv;
    }
    u32 lrows = 0;
    if (__builtin_expect(__any(send - sbeg > 640u), 0)) {
        lrows = (max((dsc >> 16) & 0x7fffu, 128u) - 128u + 63u) >> 6;
#pragma unroll
        for (int d = 1; d < 32; d <<= 1) lrows += __shfl_xor(lrows, d);
    }
    asm volatile("s_waitcnt lgkmcnt(0)" ::: "memory");
    if (lane == 0) {
        misc[wave] = (nlate + ntake) | (nlate + nov) << 16;
        misc[16 + wave] = lrows;
    }
    asm volatile("" :: "v"(code1));
    if (dsc & 0x8000u) misc[kFail] = 1;
    __syncthreads();
    u32x4 P[10];
    auto read_bins = [&](int k) { P[k] = *(LDS u32x4*)(sm + kBinOff + k * 16384 + (k < 9 ? tid : min(tid, 12 * 64 + lane)) * 16); };
    auto rm = mk_rsrc(m + (side ? 0 : kHalf * 4), kHalf * 8);
    u32x2 X[6];
    auto pay = [&](auto s) {
        if constexpr (s.v < 36) return P[s.v >> 2][s.v & 3];
        else return X[s.v - 36].x;
    };
    u32x2 md[kAhead];
    auto gather = [&](auto s) {
        if constexpr (s.v >= 36) md[s.v % kAhead] = __builtin_amdgcn_raw_buffer_load_b64(rm, X[s.v - 36].y, 0, 0);
        else md[s.v % kAhead] = __builtin_amdgcn_raw_buffer_load_b64(rm, in8(pay(s)), ((wave + (s.v >> 2) * 16) * 4 + (s.v & 3)) * 2048, 0);
        FENCE();
    };
    {
        auto rx = mk_rsrc(ws + kOffXch + b * kXchBytes, kXchBytes);
        __builtin_amdgcn_raw_buffer_store_b128(u32x4{0, 0, 0, 0}, rx, tid * 16, 0, kSc1);
        __builtin_amdgcn_raw_buffer_store_b128(u32x4{0, 0, 0, 0}, rx, tid * 16 + 16384, 0, kSc1);
        FENCE();
    }
    const u32 cpk = misc[lane & 15], lall = misc[16 + (lane & 15)];
    u32 stat = max(*(LDS u16*)(par + (lane & 15) * 2), *(LDS u16*)(par + 32 + (lane & 15) * 2));
    const u32 below = misc[kCol + (lane & 3)];
    const u32x2 flags = *(LDS u32x2*)(misc + kFail);
#pragma unroll
    for (int k = 0; k < 10; k++) read_bins(k);
    FENCE();
    stat = max(stat, (u32)__builtin_amdgcn_update_dpp(0u, stat, 0x128, 0xf, 0xf, false)) & 0x7fffu;
    const u32 other = __builtin_amdgcn_update_dpp(0u, stat, 0x124, 0xf, 0xf, false);
    const u32 mtop = (lane & 4) ? other : stat, mlow = 0x7fffu - ((lane & 4) ? stat : other);
    const u32 mex = mtop ? (mtop + 127u) >> 7 : 127u;
    u32 wide = mtop && (mex << 7) + below > mlow + kSpread ? 1u : 0u;
    const bool precise = __any(wide != 0);
    const bool unfit = __any((flags.x | flags.y) != 0 || mex - 97u > 60u);
    const u32 cn = cpk & 0xffffu, call = cpk >> 16;
    asm volatile("" :: "v"(P[0]), "v"(P[1]), "v"(P[2]), "v"(P[3]), "v"(P[4]), "v"(P[5]), "v"(P[6]), "v"(P[7]), "v"(P[8]), "v"(P[9]));
    __syncthreads();
    if (unfit) misc[kFail] = 1;
    if (__builtin_expect(!unfit, 1)) {
        {
            *(LDS u32x4*)(sm + tid * 16) = ctab;
            *(LDS u32x4*)(sm + kRbfOff + lrow(0) * 16) = rbs[0];
            if (loc(1) < kBE) *(LDS u32x4*)(sm + kRbfOff + lrow(1) * 16) = rbs[1];
            if (wave >= 8 && loc(2) < kBE) *(LDS u32x4*)(sm + kRbfOff + lrow(2) * 16) = rbs[2];
            FENCE();
            gather(IC<0>{});
            clear_sums(wave);
            FENCE();
        }
        gather(IC<1>{});
        gather(IC<2>{});
        u32 inc = cn;
        static_for<0, 4>([&](auto d) { inc += __builtin_amdgcn_update_dpp(0u, inc, 0x110 + (1 << d.v), 0xf, 0xf, true); });
        const bool fits = inc <= kLstCap;
        const u32 nfit = __builtin_popcount((u32)__builtin_amdgcn_ballot_w64(fits) & 0xffffu);
        const u32 nlist = nfit ? __builtin_amdgcn_readlane(inc, nfit - 1) : 0u;
        const u32 rest = precise ? 0u : (((fits ? call - cn : call) + 63u) >> 6) + lall;
        {
            const float sc = __uint_as_float((254u - mex) << 23);
            cs.wd = __builtin_bit_cast(s16x4, u32x2{pk_bf16(bf_lo(wdraw.x) * sc, bf_hi(wdraw.x) * sc), pk_bf16(bf_lo(wdraw.y) * sc, bf_hi(wdraw.y) * sc)});
            if ((lane & 24) == 24) cs.w = __builtin_bit_cast(bf16x8, u32x4{0, 0, 0, 0});
            cs.rbf0 = kRbfOff;
            asm volatile("" : "+v"(cs.rbf0));
        }
        float mp[4];
#pragma unroll
        for (int d = 0; d < 4; d++) mp[d] = __uint_as_float(__builtin_amdgcn_readlane(mex, d) << 23);
        if (__builtin_expect(wave < nfit, 1)) {
            const u32 first = __builtin_amdgcn_readlane(inc - cn, wave);
            if (lane < ntake) *(LDS u32x2*)(sm + kLstOff + (first + lane) * 8) = early;
            const u32 at = first + ntake + lpos - __builtin_popcount(late);
            u32 fl = late << (32 - 2 * kBufs), la = kLstOff + at * 8;
            static_for<0, kBufs>([&](auto q) { append2<(q.v < 8)>(fl, buf[(32 - kBufs + q.v) % kBufs], la); });
            asm volatile("s_waitcnt lgkmcnt(0)" ::: "memory");
            nov -= ntake;
        } else {
#pragma unroll
            for (int k = 32 - kBufs; k < 32; k += 2) {
                u64 ov[4];
#pragma unroll
                for (int q = 0; q < 4; q++) ov[q] = __builtin_amdgcn_ballot_w64((late & (1u << (2 * (31 - k) + 1 - q))) != 0);
                spill_quad(buf[k % kBufs], buf[(k + 1) % kBufs], ov, rov, nov);
            }
        }
        FENCE();
        auto fetch_listed = [&](u32 slot) {
            const u32 i = min(slot * 64 + lane, kLstCap - 1);
            u32x2 r = *(LDS u32x2*)(sm + kLstOff + i * 8);
            if (slot * 64 + lane >= nlist) r.x = 0;
            return r;
        };
        auto fix_spilled = [&](u32x2& r) {
            if (r.x == 0) r = u32x2{(lane & 15) * 8, 0};
            const u32 th = __umulhi(r.x & 0xfff8u, kThirdDiv);
            r.y = in8(r.x) + (((r.y & 255u) | (th << 8)) << 11);
            r.x += (2u - th) * kPlane;
        };
        asm volatile("s_waitcnt vmcnt(0) lgkmcnt(0)" ::: "memory");
        __syncthreads();
        __builtin_amdgcn_raw_buffer_store_b32(1u, rhs, tid == 0 ? b * 128 + 16 : kNone, 0, kSc1);
        u32x2 mv[2];
        u32 cleared;
        auto peek_cleared = [&]() {
            asm volatile("" ::: "memory");
            cleared = __builtin_amdgcn_raw_buffer_load_b32(rhs, (b ^ 1) * 128 + 16, 0, kSc1);
        };
        auto load_own = [&]() {
            peek_cleared();
            mv[0] = __builtin_amdgcn_raw_buffer_load_b64(mk_rsrc(m + (size_t)e0 * 4, kBE * 8), tid * 8, 0, 0);
            mv[1] = __builtin_amdgcn_raw_buffer_load_b64(mk_rsrc(m + (size_t)e0 * 4, kBE * 8), tid * 8 + 8192, 0, 0);
        };
        auto raw_row = [&](u32 k, u32 c) {
            auto rs = mk_rsrc(recs, __builtin_amdgcn_readlane(send, k));
            const u32 so = __builtin_amdgcn_readlane(sbeg, k) + c * 5;
            return u32x2{__builtin_amdgcn_raw_buffer_load_b32(rs, (lane >> 2) * 20 + ((0x100c0004u >> ((lane & 3) * 8)) & 255u), so, kSc1),
                         __builtin_amdgcn_raw_buffer_load_b32(rs, (lane >> 2) * 20 + 8, so, kSc1) >> ((lane & 3) * 8)};
        };
        if (__builtin_expect(!precise, 1)) {
            u32 o0 = row8(pay(IC<0>{})), o1 = row8(pay(IC<1>{})), o2 = 0;
            u32x4 rb = read_rbf<0>(o0, cs, sm);
            float c0, c1, c2;
            u32x2 ct = read_cos(pay(IC<0>{}), sm);
            static_for<3, kAhead>(gather);
            f32x16 A = radial(rb, cs);
            f32x4 Dn = down(md[0], cs);
            rb = read_rbf<0>(o1, cs, sm);
            c0 = bf_lo(ct.x); c1 = bf_hi(ct.x); c2 = __uint_as_float(ct.y);
            ct = read_cos(pay(IC<1>{}), sm);
            gather(IC<kAhead>{});
            FENCE();
            auto step = [&](auto sc) {
                constexpr int s = sc.v;
                if constexpr (s % 11 == 0) __builtin_amdgcn_s_setprio(3 - s / 11);
                asm volatile("" :: "v"(A));
                const u32x4 x = product(A, Dn, c0, c1, c2);
                FENCE();
                if constexpr (s + 1 < kSlots) {
                    A = radial(rb, cs);
                    Dn = down(md[(s + 1) % kAhead], cs);
                    c0 = bf_lo(ct.x); c1 = bf_hi(ct.x); c2 = __uint_as_float(ct.y);
                    FENCE();
                }
                if constexpr (s + 2 < kSlots) {
                    o2 = row8(pay(IC<s + 2>{}));
                    rb = read_rbf<((s + 2) >> 4)>(o2, cs, sm);
                    ct = read_cos(pay(IC<s + 2>{}), sm);
                }
                FENCE();
                deposit<(s >> 4), (s >= kSwitch)>(x, o0, sm);
                if constexpr (s + 1 + kAhead < kSlots) gather(IC<s + 1 + kAhead>{});
                FENCE();
                o0 = o1; o1 = o2;
            };
            static_for<0, 22>(step);
            if (wave + 144 < kNG) {
#pragma unroll
                for (int q = 0; q < 4; q++) X[q] = u32x2{P[9][q], (wave + 16) * 4 + q};
            } else {
#pragma unroll
                for (int q = 0; q < 4; q++) X[q] = fetch_listed((wave - 13) * 4 + q);
            }
            X[4] = fetch_listed(12 + wave);
            X[5] = fetch_listed(28 + wave);
#pragma unroll
            for (int q = 0; q < 6; q++) fix_spilled(X[q]);
            load_own();
            FENCE();
            static_for<22, kSlots - 1>(step);
            if ((28 + wave) * 64 < nlist) step(IC<kSlots - 1>{});
        }
        asm volatile("" : "+v"(wide));
        if (__builtin_expect(__any(wide != 0), 0)) {
            load_own();
#pragma unroll 1
            for (u32 k = 0; k < 32; k++) {
#pragma unroll 1
                for (u32 c = 0; __builtin_amdgcn_readlane(sbeg, k) + c * 5 < __builtin_amdgcn_readlane(send, k); c += 64) {
                    u32x2 r = raw_row(k, c);
                    fix_spilled(r);
                    const f32x4 Dn = down(__builtin_amdgcn_raw_buffer_load_b64(rm, r.y, 0, 0), cs);
                    const u32x2 ct = read_cos(r.x, sm);
                    const u32 o = row8(r.x);
                    const f32x16 A = radial(read_rbf<2>(o, cs, sm), cs);
#pragma unroll
                    for (int d = 0; d < 4; d++) {
                        const float g = fmaf(__uint_as_float(ct.y), A[8 + d], fmaf(bf_hi(ct.x), A[4 + d], bf_lo(ct.x) * A[d]));
                        __hip_atomic_fetch_add((LDS double*)(sm + kAggOff + (o - 2 * kPlane) * 4 + d * 8), (double)Dn[d] * (double)g,
                                               __ATOMIC_RELAXED, __HIP_MEMORY_SCOPE_WORKGROUP);
                    }
                }
            }
        }
        f32x2 acc[3][2] = {{{0.f, 0.f}, {0.f, 0.f}}, {{0.f, 0.f}, {0.f, 0.f}}, {{0.f, 0.f}, {0.f, 0.f}}};
        if (__builtin_expect(__any(rest != 0), 0)) {
            u32 it = kSwitch - 1, sk = 0, sc = 128, i0 = 0;
#pragma unroll 1
            do {
                if (++it == kSwitch) {
                    it = 0;
                    __syncthreads();
                    drain_row(sm, lrow(0), acc[0], true);
                    if (loc(1) < kBE) drain_row(sm, lrow(1), acc[1], true);
                    if (wave >= 8 && loc(2) < kBE) drain_row(sm, lrow(2), acc[2], true);
                    __syncthreads();
                }
                u32x2 r;
                if (i0 * 64 < nov) {
                    r = __builtin_amdgcn_raw_buffer_load_b64(rov, i0 * 64 + lane < nov ? (i0 * 64 + lane) * 8 : kNone, 0, 0);
                } else {
                    while (sk < 32 && __builtin_amdgcn_readlane(sbeg, sk) + sc * 5 >= __builtin_amdgcn_readlane(send, sk)) { sk++; sc = 128; }
                    if (sk == 32) continue;
                    r = raw_row(sk, sc);
                    sc += 64;
                }
                fix_spilled(r);
                const u32x2 g = __builtin_amdgcn_raw_buffer_load_b64(rm, r.y, 0, 0);
                const u32x2 ct = read_cos(r.x, sm);
                const u32 o = row8(r.x);
                deposit<2, 1>(product(radial(read_rbf<2>(o, cs, sm), cs), down(g, cs), bf_lo(ct.x), bf_hi(ct.x), __uint_as_float(ct.y)), o, sm);
            } while (__any(++i0 < rest));
        }
        u32x2 wu;
        {
            u32 w[4];
#pragma unroll
            for (int r = 0; r < 4; r++) w[r] = *(LDS u16*)(par + 288 + r * 8 + (lane & 3) * 2);
            wu = u32x2{w[0] | w[1] << 16, w[2] | w[3] << 16};
            asm volatile("" : "+v"(wu));
        }
        auto rin = mk_rsrc(ws + kOffXch + (b ^ 1) * kXchBytes, kBE * 16);
        u32x4 got[2];
        auto look = [&]() {
            asm volatile("" ::: "memory");
            got[0] = __builtin_amdgcn_raw_buffer_load_b128(rin, tid * 16, 0, kSc1);
            got[1] = __builtin_amdgcn_raw_buffer_load_b128(rin, tid * 16 + 16384, 0, kSc1);
            FENCE();
        };
        auto rout = mk_rsrc(ws + kOffXch + b * kXchBytes, kBE * 16);
        auto send_row = [&](int q, u32 at) {
            const u32x4 v = {__float_as_uint(acc[q][0].x), __float_as_uint(acc[q][0].y), __float_as_uint(acc[q][1].x), __float_as_uint(acc[q][1].y)};
            __builtin_amdgcn_raw_buffer_store_b128(v | 1u, rout, at, 0, kSc1);
            FENCE();
        };
        f32x2 mm[2][2], hh[2][2], sc[2][2];
        auto prepare = [&](int q, u32x3 g) {
#pragma unroll
            for (int h = 0; h < 2; h++) {
                mm[q][h] = f32x2{bf_lo(mv[q][h]), bf_hi(mv[q][h])};
                hh[q][h] = f32x2{bf_lo(g[h]), bf_hi(g[h])};
                sc[q][h] = f32x2{mp[2 * h], mp[2 * h + 1]} * __uint_as_float(g.z);
                asm volatile("" : "+v"(mm[q][h]), "+v"(hh[q][h]), "+v"(sc[q][h]));
            }
        };
        auto finish = [&](int q) {
            const u32x4 t = got[q] & ~1u;
            const f32x2 a0 = (acc[q][0] + f32x2{__uint_as_float(t.x), __uint_as_float(t.y)}) * sc[q][0];
            const f32x2 a1 = (acc[q][1] + f32x2{__uint_as_float(t.z), __uint_as_float(t.w)}) * sc[q][1];
            const f32x4 z = {0, 0, 0, 0};
            const f32x4 u = __builtin_amdgcn_mfma_f32_4x4x4bf16_1k(
                __builtin_bit_cast(s16x4, wu), __builtin_bit_cast(s16x4, u32x2{pk_bf16(a0.x, a0.y), pk_bf16(a1.x, a1.y)}), z, 0, 0, 0);
            u32x2 o;
#pragma unroll
            for (int h = 0; h < 2; h++) {
                const f32x2 p = f32x2{rnd_bf16(u[2 * h]), rnd_bf16(u[2 * h + 1])} * hh[q][h];
                const f32x2 r = mm[q][h] + f32x2{rnd_bf16(p.x), rnd_bf16(p.y)};
                o[h] = pk_bf16(r.x, r.y);
            }
            const u32x4 w = {o.x, o.y, (u32)__builtin_amdgcn_update_dpp(0u, o.x, 0xf5, 0xf, 0xf, false), (u32)__builtin_amdgcn_update_dpp(0u, o.y, 0xf5, 0xf, 0xf, false)};
            __builtin_amdgcn_raw_buffer_store_b128(w, mk_rsrc(ar.out + (size_t)e0 * 4, kBE * 8), (lane & 1) ? kNone : tid * 8 + q * 8192, 0, kSc1);
        };
        u32 row0 = lrow(0), row1 = lrow(1), row2 = lrow(2), at1 = mine1 ? kNone : loc(1) * 16, at2 = loc(2) * 16;
        asm volatile("" : "+v"(row0), "+v"(row1), "+v"(row2), "+v"(at1), "+v"(at2));
        u32 spins = 0;
        if (__builtin_expect(__builtin_amdgcn_readfirstlane(cleared) != 1u, 0)) {
#pragma unroll 1
            do {
                __builtin_amdgcn_s_sleep(8);
                peek_cleared();
            } while (__builtin_amdgcn_readfirstlane(cleared) != 1u && ++spins <= kSpinCap);
        }
        look();
        if (wave >= 4) __builtin_amdgcn_s_setprio(2);
        if (wave >= 8) __builtin_amdgcn_s_setprio(3);
        __syncthreads();
        if (__builtin_expect(precise, 0)) {
#pragma unroll
            for (int d = 0; d < 4; d++) {
                acc[0][d >> 1][d & 1] = (float)*(LDS double*)(sm + kAggOff + row0 * 32 + d * 8);
                acc[1][d >> 1][d & 1] = (float)*(LDS double*)(sm + kAggOff + row1 * 32 + d * 8);
                acc[2][d >> 1][d & 1] = (float)*(LDS double*)(sm + kAggOff + row2 * 32 + d * 8);
            }
            if (wave >= 8) send_row(2, at2);
            send_row(1, at1);
        } else {
            if (wave >= 8) {
                drain_row(sm, row2, acc[2], false);
                send_row(2, at2);
            }
            drain_row(sm, row1, acc[1], false);
            send_row(1, at1);
            __builtin_amdgcn_s_setprio(0);
            drain_row(sm, row0, acc[0], false);
        }
        prepare(0, gate0);
        if (mine1) prepare(1, u32x3{rbs[2].x, rbs[2].y, rbs[2].z});
        const u32 absent = tid + 1024 < kBE ? 0u : 1u;
#pragma unroll 1
        while (__any(!(got[0].x & got[0].y & got[0].z & got[0].w & ((got[1].x & got[1].y & got[1].z & got[1].w) | absent) & 1u)) && ++spins <= kSpinCap)
            look();
        if (__builtin_expect(spins <= kSpinCap, 1)) {
            finish(0);
            if (mine1) finish(1);
        } else {
            misc[kFail] = 1;
        }
    }
    __syncthreads();
    if (__builtin_expect(__builtin_amdgcn_readfirstlane(misc[kFail]) != 0, 0)) {
        DRAIN();
        auto ka = (const __attribute__((address_space(4))) Args*)__builtin_amdgcn_kernarg_segment_ptr();
        asm volatile("" : "+s"(ka));
        slow_bucket(sm, e0, kBE, kT, GPTR(ka->m), GPTR(ka->idx_in), GPTR(ka->idx_out), GPTR(ka->dist), GPTR(ka->angle), GPTR(ka->centers),
                    GPTR(ka->freq), GPTR(ka->w_rbf), GPTR(ka->w_sbf), GPTR(ka->w_down), GPTR(ka->w_up), GPTR(ka->out));
    }
}

__global__ void __launch_bounds__(1024) k_generic(u32 E, u32 T, const u16* __restrict__ m, const int* __restrict__ idx_in,
                                                  const int* __restrict__ idx_out, const u16* __restrict__ dist,
                                                  const u16* __restrict__ angle, const u16* __restrict__ centers,
                                                  const u16* __restrict__ freq, const u16* __restrict__ w_rbf,
                                                  const u16* __restrict__ w_sbf, const u16* __restrict__ w_down,
                                                  const u16* __restrict__ w_up, u16* __restrict__ out) {
    __shared__ __attribute__((aligned(16))) char smem_[kBE * 64];
    asm volatile(".Lgm_next:");
    const u32 e0 = blockIdx.x * kBE;
    slow_bucket((LDS char*)smem_, e0, min(kBE, E - e0), T, m, idx_in, idx_out, dist, angle, centers, freq, w_rbf, w_sbf, w_down, w_up, out);
}

}

extern "C" void kernel_launch(void* const* d_in, const int* in_sizes, int n_in, void* d_out, int out_size, void* d_ws,
                              size_t ws_size, hipStream_t stream) {
    const u16* m = (const u16*)d_in[0];
    const int* idx_in = (const int*)d_in[1];
    const int* idx_out = (const int*)d_in[2];
    const u16* dist = (const u16*)d_in[3];
    const u16* angle = (const u16*)d_in[4];
    const u16* centers = (const u16*)d_in[5];
    const u16* freq = (const u16*)d_in[6];
    const u16* w_rbf = (const u16*)d_in[7];
    const u16* w_sbf = (const u16*)d_in[8];
    const u16* w_down = (const u16*)d_in[9];
    const u16* w_up = (const u16*)d_in[10];
    u16* out = (u16*)d_out;
    const u32 E = (u32)in_sizes[3], T = (u32)in_sizes[1];
    if (E == kE && T == kT && ws_size >= kOffEnd) {
        const Args ar = {idx_in, idx_out, angle, m, (char*)d_ws, dist, w_sbf, w_rbf, w_down, w_up, centers, freq, out};
        k_fused<<<kNB, 1024, 0, stream>>>(ar);
    } else {
        k_generic<<<(E + kBE - 1) / kBE, 1024, 0, stream>>>(E, T, m, idx_in, idx_out, dist, angle, centers, freq, w_rbf, w_sbf, w_down, w_up, out);
    }
}
```
